```python
import torch

def _softmax(x, axis=-1):
    return torch.softmax(x, dim=axis)
def _gelu(x):
    return torch.nn.functional.gelu(x, approximate="tanh")
def _silu(x):
    return torch.nn.functional.silu(x)
def _sigmoid(x):
    return torch.sigmoid(x)
def _layernorm(x, axis=-1, eps=1e-5):
    m = x.mean(dim=axis, keepdim=True)
    v = x.var(dim=axis, keepdim=True, unbiased=False)
    return (x - m) / torch.sqrt(v + eps)
def _scatter_add(tgt, idx, src, axis=0):
    return tgt.clone().index_add_(axis, idx, src)
def _segment_sum(src, seg, n):
    out = torch.zeros((n,) + src.shape[1:], dtype=src.dtype)
    return out.index_add_(0, seg, src)
def _pad(x, pw):
    flat = tuple(v for pair in reversed(pw) for v in pair)
    return torch.nn.functional.pad(x, flat)

kernel_name = 'interleave_layers__sliding_block__mixer_channel_mlp__prompt'

def setup_inputs():

    _w = (lambda a: (lambda t: t.to(torch.bfloat16) if t.is_floating_point() else t)(torch.as_tensor(a)))
    def _A_setup():
        B, S, D = 1, 2048, 448
        return {
            "x": _w((lambda *s: torch.randn(*s, dtype=torch.bfloat16) * 0.05)(B, S, D)),
            "wqkv": _w((lambda *s: torch.randn(*s, dtype=torch.bfloat16) * 0.05)(D, 3 * D)),
            "wo": _w((lambda *s: torch.randn(*s, dtype=torch.bfloat16) * 0.05)(D, D)),
        }
    _A_in = _A_setup()

    def _B_setup():
        B, S, D, F = 1, 2048, 448, 1792
        return {
            "x": _w((lambda *s: torch.randn(*s, dtype=torch.bfloat16) * 0.05)(B, S, D)),
            "w1": _w((lambda *s: torch.randn(*s, dtype=torch.bfloat16) * 0.8)(D, F) * (D ** -0.5)),
            "b1": _w((lambda *s: torch.randn(*s, dtype=torch.bfloat16) * 0.8)(F)),
            "w2": _w((lambda *s: torch.randn(*s, dtype=torch.bfloat16) * 0.8)(F, D) * (F ** -0.5)),
            "b2": _w((lambda *s: torch.randn(*s, dtype=torch.bfloat16) * 0.4)(D)),
        }
    _B_in = _B_setup()

    return {
        "A_x": _A_in["x"],
        "A_wqkv": _A_in["wqkv"],
        "A_wo": _A_in["wo"],
        "B_x": _B_in["x"],
        "B_w1": _B_in["w1"],
        "B_b1": _B_in["b1"],
        "B_w2": _B_in["w2"],
        "B_b2": _B_in["b2"],
    }

def reference(A_x, A_wqkv, A_wo, B_x, B_w1, B_b1, B_w2, B_b2):
    def _A_ref(x, wqkv, wo):
        B, S, D = x.shape
        H = 16; Dh = D // H; W = 256
        qkv = (lambda x, *d: x.permute(*d))((x @ wqkv).reshape(B, S, 3, H, Dh), 2, 0, 3, 1, 4)
        q, k, v = qkv[0], qkv[1], qkv[2]
        scores = torch.einsum("bhqd,bhkd->bhqk", q, k) * (Dh ** -0.5)
        idx = torch.arange(S)
        mask = (idx[:, None] - idx[None, :] >= 0) & (idx[:, None] - idx[None, :] < W)
        scores = torch.where(mask[None, None], scores, -1e9)
        p = _softmax(scores, axis=-1)
        o = torch.einsum("bhqk,bhkd->bhqd", p, v)
        return (lambda x, *d: x.permute(*d))(o, 0, 2, 1, 3).reshape(B, S, D) @ wo

    def _B_ref(x, w1, b1, w2, b2):
        y = _layernorm(x, axis=-1)
        h = _gelu(torch.einsum("bsd,df->bsf", y, w1) + b1)
        return torch.einsum("bsf,fd->bsd", h, w2) + b2

    x = A_x
    x = x + _A_ref(_layernorm(x, axis=-1), A_wqkv, A_wo)
    x = x + _B_ref(_layernorm(x, axis=-1), B_w1, B_b1, B_w2, B_b2)
    x = x + _A_ref(_layernorm(x, axis=-1), A_wqkv, A_wo)
    x = x + _B_ref(_layernorm(x, axis=-1), B_w1, B_b1, B_w2, B_b2)
    return x
```

```cpp
#include <hip/hip_runtime.h>
#include <hip/hip_bf16.h>
typedef unsigned short u16;
__device__ __forceinline__ float bf2f(u16 b){ return __uint_as_float(((unsigned)b)<<16); }
__device__ __forceinline__ u16 f2bf(float f){ __bf16 h=(__bf16)f; return __builtin_bit_cast(u16,h); }
constexpr int S=2048,D=448,H=16,DH=28,W=256,F=1792;

__global__ void k_ln(const u16* x, u16* y, int twice){
  int r=blockIdx.x; int l=threadIdx.x;
  float v[7]; float s=0;
  for(int i=0;i<7;i++){ v[i]=bf2f(x[r*D+l+64*i]); s+=v[i]; }
  for(int rep=0;rep<=twice;rep++){
    s=0; for(int i=0;i<7;i++) s+=v[i];
    for(int o=32;o;o>>=1) s+=__shfl_xor(s,o);
    float m=s/D; float q=0; for(int i=0;i<7;i++){ float d=v[i]-m; q+=d*d; }
    for(int o=32;o;o>>=1) q+=__shfl_xor(q,o);
    float rs=rsqrtf(q/D+1e-5f);
    for(int i=0;i<7;i++) v[i]=(v[i]-m)*rs;
  }
  for(int i=0;i<7;i++) y[r*D+l+64*i]=f2bf(v[i]);
}
__global__ void k_gemm(const u16* A,const u16* B,u16* C,const u16* bias,const u16* res,int N,int K,int gelu){
  __shared__ float As[16][17], Bs[16][17];
  int tx=threadIdx.x&15, ty=threadIdx.x>>4;
  int row=blockIdx.y*16+ty, col=blockIdx.x*16+tx;
  float acc=0;
  for(int k0=0;k0<K;k0+=16){
    As[ty][tx]=bf2f(A[row*K+k0+tx]); Bs[ty][tx]=bf2f(B[(k0+ty)*N+col]);
    __syncthreads();
    for(int k=0;k<16;k++) acc+=As[ty][k]*Bs[k][tx];
    __syncthreads();
  }
  if(bias) acc+=bf2f(bias[col]);
  if(gelu){ float u=0.7978845608f*(acc+0.044715f*acc*acc*acc); acc=0.5f*acc*(1.f+tanhf(u)); }
  if(res) acc+=bf2f(res[row*N+col]);
  C[row*N+col]=f2bf(acc);
}
__global__ void k_attn(const u16* qkv,u16* o){
  int q=blockIdx.x, h=blockIdx.y, t=threadIdx.x;
  __shared__ float p[256]; __shared__ float red[256];
  int key=q-t; float s=-1e30f;
  if(key>=0){ s=0; for(int d=0;d<DH;d++) s+=bf2f(qkv[q*3*D+h*DH+d])*bf2f(qkv[key*3*D+D+h*DH+d]); s*=rsqrtf((float)DH);}
  red[t]=s; __syncthreads();
  for(int o2=128;o2;o2>>=1){ if(t<o2) red[t]=fmaxf(red[t],red[t+o2]); __syncthreads(); }
  float m=red[0]; __syncthreads();
  float e=key>=0?__expf(s-m):0.f; p[t]=e; red[t]=e; __syncthreads();
  for(int o2=128;o2;o2>>=1){ if(t<o2) red[t]+=red[t+o2]; __syncthreads(); }
  float sum=red[0];
  if(t<DH){ float a=0; for(int j=0;j<256;j++){ int kk=q-j; if(kk>=0) a+=p[j]*bf2f(qkv[kk*3*D+2*D+h*DH+t]); } o[q*D+h*DH+t]=f2bf(a/sum); }
}
extern "C" void kernel_launch(void* const* d_in, const int* in_sizes, int n_in, void* d_out, int out_size, void* d_ws, size_t ws_size, hipStream_t stream){
  const u16* x=(const u16*)d_in[0]; const u16* wqkv=(const u16*)d_in[1]; const u16* wo=(const u16*)d_in[2];
  const u16* w1=(const u16*)d_in[4]; const u16* b1=(const u16*)d_in[5]; const u16* w2=(const u16*)d_in[6]; const u16* b2=(const u16*)d_in[7];
  u16* ws=(u16*)d_ws; u16* y=ws; u16* qkv=y+S*D; u16* o=qkv+S*3*D; u16* x1=o+S*D; u16* h=x1+S*D; u16* x2=h+S*F;
  const u16* cur=x;
  for(int l=0;l<2;l++){
    k_ln<<<S,64,0,stream>>>(cur,y,0);
    k_gemm<<<dim3(3*D/16,S/16),256,0,stream>>>(y,wqkv,qkv,nullptr,nullptr,3*D,D,0);
    k_attn<<<dim3(S,H),256,0,stream>>>(qkv,o);
    k_gemm<<<dim3(D/16,S/16),256,0,stream>>>(o,wo,x1,nullptr,cur,D,D,0);
    k_ln<<<S,64,0,stream>>>(x1,y,1);
    k_gemm<<<dim3(F/16,S/16),256,0,stream>>>(y,w1,h,b1,nullptr,F,D,1);
    u16* dst= l==0? x2 : (u16*)d_out;
    k_gemm<<<dim3(D/16,S/16),256,0,stream>>>(h,w2,dst,b2,x1,D,F,0);
    cur=x2;
  }
}
```

```cpp
#include <hip/hip_runtime.h>
#include <stdint.h>

#define GAS __attribute__((address_space(1)))
#define LAS __attribute__((address_space(3)))
#define DI __device__ __forceinline__
typedef unsigned v4u __attribute__((ext_vector_type(4)));
typedef unsigned v2u __attribute__((ext_vector_type(2)));
typedef float f32x4 __attribute__((ext_vector_type(4)));
typedef float f32x2 __attribute__((ext_vector_type(2)));
typedef __bf16 bf16x8 __attribute__((ext_vector_type(8)));
typedef __bf16 bf16x2 __attribute__((ext_vector_type(2)));
typedef short s16x4 __attribute__((ext_vector_type(4)));
typedef unsigned long long u64;
typedef unsigned short u16;
typedef __amdgpu_buffer_rsrc_t rsrc_t;

namespace {
constexpr int S = 2048, D = 448, F = 1792;
constexpr int NWG = 256, NTHR = 512;
constexpr float EPS = 1e-5f;
constexpr int AUX_SC1 = 16;
constexpr unsigned SPIN_CAP = 1u << 16;

constexpr int OFF_EPOCH = 0;
constexpr int OFF_PREPF = 1024;
constexpr int OFF_FLAGS = 8192, FLAGS_L = 81920;
constexpr int FL_KV = 0, FL_O = 4096, FL_X1 = 4096 + 16384, FL_H = 4096 + 32768, FL_X2 = 4096 + 49152;
constexpr int OFF_WT = 262144;
constexpr int ID_WQ = 0, ID_WO = 1344, ID_W1 = 1792, ID_W2 = 3360, ID_END = 4928;
constexpr int OFF_CSP = OFF_WT + ID_END * 1024;
constexpr int OFF_KV = OFF_CSP + ID_END * 64 + 3072;
constexpr int KV_L = 16 * 128 * 2048;
constexpr int OFF_O = OFF_KV + 2 * KV_L;
constexpr int O_L = 128 * 16 * 1024;
constexpr int OFF_X1 = OFF_O + 2 * O_L;
constexpr int X_L = 128 * 14 * 1024;
constexpr int OFF_H = OFF_X1 + 2 * X_L;
constexpr int H_L = 128 * 56 * 1024;
constexpr int OFF_X2 = OFF_H + 2 * H_L;
constexpr int OFF_STAMP = OFF_X2 + X_L;
constexpr int WS_BYTES = OFF_STAMP + 256 * 64 * 8;
static_assert(OFF_KV % 1024 == 0, "align");

constexpr int L_X = 0;
constexpr int L_Y = 100352;
constexpr int L_C = L_Y + 49152;
constexpr int LC_CSQ = L_C, LC_CS1 = L_C + 512, LC_B1 = L_C + 1024;
constexpr int LDS_BYTES = L_C + 2048;

DI f32x4 mfma32(v4u a, v4u b, f32x4 c) {
  return __builtin_amdgcn_mfma_f32_16x16x32_bf16(__builtin_bit_cast(bf16x8, a), __builtin_bit_cast(bf16x8, b), c, 0, 0, 0);
}
DI f32x4 mfma16(v2u a, v2u b, f32x4 c) {
  return __builtin_amdgcn_mfma_f32_16x16x16bf16_1k(__builtin_bit_cast(s16x4, a), __builtin_bit_cast(s16x4, b), c, 0, 0, 0);
}
DI unsigned pk2(float a, float b) {
  f32x2 v = {a, b};
  return __builtin_bit_cast(unsigned, __builtin_convertvector(v, bf16x2));
}
DI float bflo(unsigned u) { return __builtin_bit_cast(float, u << 16); }
DI float bfhi(unsigned u) { return __builtin_bit_cast(float, u & 0xFFFF0000u); }
DI float dot2(unsigned a, unsigned b, float c) {
  return __builtin_amdgcn_fdot2_f32_bf16(__builtin_bit_cast(bf16x2, a), __builtin_bit_cast(bf16x2, b), c, false);
}
DI float gsum(float x) {
  auto r = __builtin_amdgcn_permlane32_swap(__builtin_bit_cast(unsigned, x), __builtin_bit_cast(unsigned, x), false, false);
  x = __uint_as_float(r[0]) + __uint_as_float(r[1]);
  auto q = __builtin_amdgcn_permlane16_swap(__builtin_bit_cast(unsigned, x), __builtin_bit_cast(unsigned, x), false, false);
  return __uint_as_float(q[0]) + __uint_as_float(q[1]);
}
DI float gmax(float x, float floor_) {
  auto r = __builtin_amdgcn_permlane32_swap(__builtin_bit_cast(unsigned, x), __builtin_bit_cast(unsigned, x), false, false);
  x = fmaxf(fmaxf(floor_, __uint_as_float(r[0])), __uint_as_float(r[1]));
  auto q = __builtin_amdgcn_permlane16_swap(__builtin_bit_cast(unsigned, x), __builtin_bit_cast(unsigned, x), false, false);
  return fmaxf(fmaxf(floor_, __uint_as_float(q[0])), __uint_as_float(q[1]));
}
DI v4u ld16(rsrc_t rs, int voff, int soff) { return __builtin_amdgcn_raw_buffer_load_b128(rs, voff, soff, AUX_SC1); }
DI void st16(v4u v, rsrc_t rs, int voff, int soff) { __builtin_amdgcn_raw_buffer_store_b128(v, rs, voff, soff, AUX_SC1); }
DI int opq(int v) { asm volatile("" : "+s"(v)); return v; }
DI int opqv(int v) { asm volatile("" : "+v"(v)); return v; }
#define DRAIN() asm volatile("s_waitcnt vmcnt(0)" ::: "memory")
#define BARRIER() asm volatile("s_waitcnt lgkmcnt(0)\n\ts_barrier" ::: "memory")
DI void dma1k(rsrc_t rs, int voff, int soff, unsigned ldsaddr) {
  asm volatile("s_mov_b32 m0, %0\n\ts_nop 0\n\tbuffer_load_dwordx4 %1, %2, %3 offen sc1 lds" ::"s"(ldsaddr), "v"(voff), "s"(rs), "s"(soff)
               : "memory");
}
DI void wait_flags(const unsigned char *ws, int off, u64 tag) {
  const GAS u64 *p = (const GAS u64 *)(ws + off);
  for(unsigned spin = 0; spin < SPIN_CAP; ++spin) {
    const u64 v = __hip_atomic_load(p, __ATOMIC_RELAXED, __HIP_MEMORY_SCOPE_AGENT);
    if(__all(v == tag)) break;
    __builtin_amdgcn_s_sleep(1);
  }
  __builtin_amdgcn_fence(__ATOMIC_ACQUIRE, "wavefront");
}
DI void set_flag(unsigned char *ws, int off, u64 tag) {
  __hip_atomic_store((GAS u64 *)(ws + off), tag, __ATOMIC_RELAXED, __HIP_MEMORY_SCOPE_AGENT);
}

#define STAMP(i)

template <bool FIRST, int MASK>
DI void attn_chunk(LAS unsigned char *kv, int lane, int lo, int hi, int ms, v4u qf, float &m, f32x4 &o0, f32x4 &o1) {
  const int g = lane >> 4, ql = lane & 15;
  f32x4 s[8];
  float mx = -1e30f;
#pragma unroll
  for(int c = 0; c < 8; ++c) {
    if(c >= lo && c < hi) {
      const v4u kf = *(LAS v4u *)(kv + c * 2048 + lane * 16);
      f32x4 z = {0.f, 0.f, 0.f, 0.f};
      s[c] = mfma32(kf, qf, z);
      if(MASK != 0 && c == ms) {
#pragma unroll
        for(int r = 0; r < 4; ++r) {
          const int kl = 4 * g + r;
          const bool ok = MASK == 1 ? (kl <= ql) : (kl > ql);
          s[c][r] = ok ? s[c][r] : -1e30f;
        }
      }
      mx = fmaxf(fmaxf(mx, s[c][0]), s[c][1]);
      mx = fmaxf(fmaxf(mx, s[c][2]), s[c][3]);
    }
  }
  const float mn = gmax(mx, FIRST ? -1e30f : m);
  if(!FIRST) {
    const float al = __builtin_amdgcn_exp2f(m - mn);
    o0 *= al;
    o1 *= al;
  }
  m = mn;
#pragma unroll
  for(int c = 0; c < 8; ++c) {
    if(c >= lo && c < hi) {
      const v4u vf = *(LAS v4u *)(kv + c * 2048 + 1024 + lane * 16);
      v2u p;
      p.x = pk2(__builtin_amdgcn_exp2f(s[c][0] - mn), __builtin_amdgcn_exp2f(s[c][1] - mn));
      p.y = pk2(__builtin_amdgcn_exp2f(s[c][2] - mn), __builtin_amdgcn_exp2f(s[c][3] - mn));
      o0 = mfma16(v2u{vf.x, vf.y}, p, o0);
      o1 = mfma16(v2u{vf.z, vf.w}, p, o1);
    }
  }
}

__global__ void __launch_bounds__(NTHR, 2)
    fused_kernel(const u16 *__restrict__ x, const u16 *__restrict__ wqkv, const u16 *__restrict__ wo, const u16 *__restrict__ w1,
        const u16 *__restrict__ b1, const u16 *__restrict__ w2, const u16 *__restrict__ b2, u16 *__restrict__ out, unsigned char *ws) {
  __shared__ __attribute__((aligned(16))) unsigned char smem[LDS_BYTES];
  LAS unsigned char *L = (LAS unsigned char *)smem;
  const unsigned ldsb = (unsigned)(size_t)L;
  const int tid = threadIdx.x, w0 = __builtin_amdgcn_readfirstlane(tid >> 6), b0 = blockIdx.x;
#define IDS() \
  const int lane = opqv(tid) & 63, w = opq(w0), b = opq(b0), t = (b & 7) * 2 + (b >> 7), slot = (b >> 3) & 15; \
  const int tw = w < 4 ? w : 11 - w, tt = 8 * t + tw, g = lane >> 4, li = lane & 15; \
  (void)g, (void)li, (void)tt, (void)slot
#define XOPERAND() \
  const rsrc_t rx = layer == 0 ? __builtin_amdgcn_make_buffer_rsrc((void *)x, 0, S * D * 2, 0x00020000) : rws; \
  const int xvo = layer == 0 ? (16 * tt + li) * (D * 2) + g * 16 : OFF_X2 + tt * 14336 + lane * 16; \
  const int xst = opq(layer == 0 ? 64 : 1024)
  const rsrc_t rws = __builtin_amdgcn_make_buffer_rsrc((void *)ws, 0, WS_BYTES, 0x00020000);
  STAMP(0);
  u64 tag = __hip_atomic_load((const GAS u64 *)(ws + OFF_EPOCH), __ATOMIC_RELAXED, __HIP_MEMORY_SCOPE_AGENT) + 1ull;
  tag = ((u64)(unsigned)__builtin_amdgcn_readfirstlane((unsigned)(tag >> 32)) << 32) | (unsigned)__builtin_amdgcn_readfirstlane((unsigned)tag);

  {
  IDS();
#pragma unroll 1
  for(int id = b * 8 + w; id < ID_END; id += NWG * 8) {
    const u16 *src;
    int bytes, ld, rowA, rowB, col;
    bool okA = true, okB = true;
    const int pc = 8 * (li >> 2) + (li & 3);
    if(id < ID_WO) {
      const int head = id / 84, rem = id % 84, ks = rem / 6, nt = rem % 6, dc = pc + 4 * (nt & 1);
      src = wqkv, bytes = D * 3 * D * 2, ld = 3 * D, rowA = 32 * ks + 8 * g, rowB = rowA + 4;
      col = (nt >> 1) * D + 28 * head + dc;
      okA = okB = dc < 28;
    } else if(id < ID_W1) {
      const int i2 = id - ID_WO, sl = i2 >> 5, ks = (i2 & 31) >> 1, e = i2 & 1;
      src = wo, bytes = D * D * 2, ld = D, rowA = 28 * ks + 8 * g, rowB = rowA + 4;
      col = 32 * sl + pc + 4 * e;
      okB = g < 3;
    } else if(id < ID_W2) {
      const int i2 = id - ID_W1, sl = i2 / 98, rem = i2 % 98, ks = rem / 7, nt = rem % 7;
      src = w1, bytes = D * F * 2, ld = F, rowA = 32 * ks + 8 * g, rowB = rowA + 4;
      col = nt < 6 ? 112 * sl + 32 * (nt >> 1) + pc + 4 * (nt & 1) : 112 * sl + 96 + li;
    } else {
      const int i2 = id - ID_W2, cs = i2 / 224, rem = i2 % 224, hks = rem >> 2, nt = rem & 3;
      src = w2, bytes = F * D * 2, ld = D;
      if(hks < 48) rowA = 112 * (hks / 3) + 32 * (hks % 3) + 8 * g, rowB = rowA + 4;
      else rowA = 224 * (hks - 48) + 96 + 4 * g, rowB = rowA + 112;
      col = 64 * cs + 32 * (nt >> 1) + pc + 4 * (nt & 1);
    }
    const rsrc_t rs = __builtin_amdgcn_make_buffer_rsrc((void *)src, 0, bytes, 0x00020000);
    const int oa = okA ? (rowA * ld + col) * 2 : 0x7F000000, ob = okB ? (rowB * ld + col) * 2 : 0x7F000000;
    unsigned e[8];
#pragma unroll
    for(int j = 0; j < 4; ++j) {
      e[j] = (unsigned)(u16)__builtin_amdgcn_raw_buffer_load_b16(rs, oa, j * ld * 2, 0);
      e[4 + j] = (unsigned)(u16)__builtin_amdgcn_raw_buffer_load_b16(rs, ob, j * ld * 2, 0);
    }
    const v4u f = {e[0] | (e[1] << 16), e[2] | (e[3] << 16), e[4] | (e[5] << 16), e[6] | (e[7] << 16)};
    st16(f, rws, OFF_WT + lane * 16, id * 1024);
    float cs = 0.f;
    cs = dot2(f.x, 0x3F803F80u, cs), cs = dot2(f.y, 0x3F803F80u, cs), cs = dot2(f.z, 0x3F803F80u, cs), cs = dot2(f.w, 0x3F803F80u, cs);
    cs = gsum(cs);
    if(lane < 16) __builtin_amdgcn_raw_buffer_store_b32(__builtin_bit_cast(unsigned, cs), rws, OFF_CSP + lane * 4, id * 64, AUX_SC1);
  }
  DRAIN();
  BARRIER();
  if(tid == 0) set_flag(ws, OFF_PREPF + b * 8, tag);
  STAMP(1);
#pragma unroll
  for(int i = 0; i < 4; ++i) wait_flags(ws, OFF_PREPF + (i * 64 + lane) * 8, tag);
  if(b == 0 && tid == 0) set_flag(ws, OFF_EPOCH, tag);
  STAMP(2);
  for(int i = w; i < 84; i += 8) dma1k(rws, lane * 16, OFF_WT + (ID_WQ + slot * 84 + i) * 1024, ldsb + L_X + i * 1024);
  if(tid < 96 + 112) {
    const bool isq = tid < 96;
    const int c = isq ? tid : tid - 96, nt = c >> 4, i = c & 15;
    const int id0 = isq ? ID_WQ + slot * 84 + nt : ID_W1 + slot * 98 + nt, stp = isq ? 6 : 7;
    float sum = 0.f;
#pragma unroll
    for(int ks = 0; ks < 14; ++ks)
      sum += __builtin_bit_cast(float, __builtin_amdgcn_raw_buffer_load_b32(rws, OFF_CSP + (id0 + ks * stp) * 64 + i * 4, 0, AUX_SC1));
    *(LAS float *)(L + (isq ? LC_CSQ : LC_CS1) + c * 4) = sum;
    if(!isq) {
      const int col = nt < 6 ? 112 * slot + 32 * (nt >> 1) + 8 * (i >> 2) + (i & 3) + 4 * (nt & 1) : 112 * slot + 96 + i;
      *(LAS float *)(L + LC_B1 + c * 4) = bflo(b1[col]);
    }
  }
  }

#pragma unroll 1
  for(int layer = 0; layer < 2; ++layer) {
    const int fl = OFF_FLAGS + layer * FLAGS_L;
    v4u qf;
    {
      IDS();
      XOPERAND();
      if(layer == 1) wait_flags(ws, OFF_FLAGS + FL_X2 + tt * 128 + (lane < 14 ? lane : 0) * 8, tag);
      v4u xf[14];
#pragma unroll
      for(int ks = 0; ks < 14; ++ks) xf[ks] = ld16(rx, xvo, ks * xst);
      __builtin_amdgcn_sched_barrier(0);
      asm volatile("s_waitcnt vmcnt(14)" ::: "memory");
      BARRIER();
      STAMP(3 + layer * 16);
      f32x4 acc[6];
#pragma unroll
      for(int n = 0; n < 6; ++n) acc[n] = f32x4{0.f, 0.f, 0.f, 0.f};
      float s1 = 0.f, s2 = 0.f;
#pragma unroll
      for(int ks = 0; ks < 14; ++ks) {
#pragma unroll
        for(int n = 0; n < 6; ++n) {
          const v4u wf = *(LAS v4u *)(L + L_X + (ks * 6 + n) * 1024 + lane * 16);
          acc[n] = n < 4 ? mfma32(wf, xf[ks], acc[n]) : mfma32(xf[ks], wf, acc[n]);
        }
#pragma unroll
        for(int j = 0; j < 4; ++j) s1 = dot2(xf[ks][j], 0x3F803F80u, s1), s2 = dot2(xf[ks][j], xf[ks][j], s2);
      }
      s1 = gsum(s1), s2 = gsum(s2);
      const float mu = s1 * (1.f / D), var = fmaxf(s2 * (1.f / D) - mu * mu, 0.f), rs = __builtin_amdgcn_rsqf(var + EPS);
      const float qs = rs * (0.18898223650461362f * 1.4426950408889634f);
      v4u kf, vf;
      {
        f32x4 c[4];
#pragma unroll
        for(int n = 0; n < 4; ++n) c[n] = *(LAS f32x4 *)(L + LC_CSQ + n * 64 + g * 16);
        f32x4 a0 = (acc[0] - mu * c[0]) * qs, a1 = (acc[1] - mu * c[1]) * qs;
        qf = v4u{pk2(a0[0], a0[1]), pk2(a0[2], a0[3]), pk2(a1[0], a1[1]), pk2(a1[2], a1[3])};
        a0 = (acc[2] - mu * c[2]) * rs, a1 = (acc[3] - mu * c[3]) * rs;
        kf = v4u{pk2(a0[0], a0[1]), pk2(a0[2], a0[3]), pk2(a1[0], a1[1]), pk2(a1[2], a1[3])};
        const float c4 = *(LAS float *)(L + LC_CSQ + 256 + li * 4), c5 = *(LAS float *)(L + LC_CSQ + 320 + li * 4);
#pragma unroll
        for(int r = 0; r < 4; ++r) {
          const float mur = __shfl(mu, 4 * g + r), rsr = __shfl(rs, 4 * g + r);
          a0[r] = (acc[4][r] - mur * c4) * rsr;
          a1[r] = li == 12 ? 1.f : (acc[5][r] - mur * c5) * rsr;
        }
        vf = v4u{pk2(a0[0], a0[1]), pk2(a0[2], a0[3]), pk2(a1[0], a1[1]), pk2(a1[2], a1[3])};
      }
      const int kvo = OFF_KV + layer * KV_L + (slot * 128 + tt) * 2048;
      st16(kf, rws, lane * 16, kvo);
      st16(vf, rws, lane * 16 + 1024, kvo);
      *(LAS v4u *)(L + L_Y + (16 + tw) * 2048 + lane * 16) = kf;
      *(LAS v4u *)(L + L_Y + (16 + tw) * 2048 + 1024 + lane * 16) = vf;
    }
    DRAIN();
    BARRIER();
    {
      IDS();
      if(tid == 0) set_flag(ws, fl + FL_KV + (slot * 16 + t) * 8, tag);
      STAMP(4 + layer * 16);
      for(int i = w; i < 98; i += 8) dma1k(rws, lane * 16, OFF_WT + (ID_W1 + slot * 98 + i) * 1024, ldsb + L_X + i * 1024);
      float m;
      f32x4 o0 = {0.f, 0.f, 0.f, 0.f}, o1 = {0.f, 0.f, 0.f, 0.f};
      attn_chunk<true, 1>(L + L_Y + 16 * 2048, lane, 0, tw + 1, tw, qf, m, o0, o1);
      STAMP(5 + layer * 16);
      if(t > 0) {
        wait_flags(ws, fl + FL_KV + (slot * 16 + ((lane & 1) && t > 1 ? t - 2 : t - 1)) * 8, tag);
        STAMP(6 + layer * 16);
        const int kvr = OFF_KV + layer * KV_L + (slot * 128 + 8 * t - 16) * 2048;
#pragma unroll
        for(int i = 0; i < 4; ++i) {
          const int pc = i * 8 + w;
          if(t > 1 || pc >= 16) dma1k(rws, lane * 16, kvr + pc * 1024, ldsb + L_Y + pc * 1024);
        }
      }
      DRAIN();
      BARRIER();
      STAMP(7 + layer * 16);
      if(t > 0) attn_chunk<false, 0>(L + L_Y + 8 * 2048, lane, 0, 8, 0, qf, m, o0, o1);
      if(t > 1) attn_chunk<false, 2>(L + L_Y, lane, tw, 8, tw, qf, m, o0, o1);
      const float inv = __builtin_amdgcn_rcpf(__shfl(o1[0], 48 + li));
      o0 *= inv, o1 *= inv;
      const v4u of = {pk2(o0[0], o0[1]), pk2(o0[2], o0[3]), pk2(o1[0], o1[1]), pk2(o1[2], o1[3])};
      st16(of, rws, lane * 16, OFF_O + layer * O_L + (tt * 16 + slot) * 1024);
      DRAIN();
      if(lane == 0) set_flag(ws, fl + FL_O + tt * 128 + slot * 8, tag);
    }
    STAMP(8 + layer * 16);
    if(((b0 >> 3) & 15) < 14) {
      IDS();
      XOPERAND();
      BARRIER();
#pragma unroll
      for(int i = 0; i < 4; ++i)
        dma1k(rws, lane * 16, OFF_WT + (ID_WO + slot * 32 + i * 8 + w) * 1024, ldsb + L_Y + (i * 8 + w) * 1024);
      const v4u xr = ld16(rx, xvo, slot * xst);
      wait_flags(ws, fl + FL_O + tt * 128 + li * 8, tag);
      STAMP(9 + layer * 16);
      v4u of[16];
      const int ob = opq(OFF_O + layer * O_L + tt * 16384);
#pragma unroll
      for(int h = 0; h < 16; ++h) of[h] = ld16(rws, lane * 16 + (h & 3) * 1024, ob + (h >> 2) * 4096);
      __builtin_amdgcn_sched_barrier(0);
      asm volatile("s_waitcnt vmcnt(16)" ::: "memory");
      BARRIER();
      f32x4 a0 = {0.f, 0.f, 0.f, 0.f}, a1 = {0.f, 0.f, 0.f, 0.f};
#pragma unroll
      for(int h = 0; h < 16; ++h) {
        a0 = mfma32(*(LAS v4u *)(L + L_Y + h * 2048 + lane * 16), of[h], a0);
        a1 = mfma32(*(LAS v4u *)(L + L_Y + h * 2048 + 1024 + lane * 16), of[h], a1);
      }
      const v4u y = {pk2(a0[0] + bflo(xr.x), a0[1] + bfhi(xr.x)), pk2(a0[2] + bflo(xr.y), a0[3] + bfhi(xr.y)),
          pk2(a1[0] + bflo(xr.z), a1[1] + bfhi(xr.z)), pk2(a1[2] + bflo(xr.w), a1[3] + bfhi(xr.w))};
      st16(y, rws, lane * 16, OFF_X1 + layer * X_L + (tt * 14 + slot) * 1024);
      DRAIN();
      if(lane == 0) set_flag(ws, fl + FL_X1 + tt * 128 + slot * 8, tag);
    }
    STAMP(10 + layer * 16);
    v4u w2f[28];
    {
      IDS();
      const int csl = slot % 7;
      wait_flags(ws, fl + FL_X1 + tt * 128 + (lane < 14 ? lane : 0) * 8, tag);
      STAMP(11 + layer * 16);
      const int xb = opq(OFF_X1 + layer * X_L + tt * 14336), wb = opq(OFF_WT + (ID_W2 + csl * 224 + w * 28) * 1024);
      v4u xf[14];
#pragma unroll
      for(int ks = 0; ks < 14; ++ks) xf[ks] = ld16(rws, lane * 16 + (ks & 3) * 1024, xb + (ks >> 2) * 4096);
#pragma unroll
      for(int i = 0; i < 28; ++i) w2f[i] = ld16(rws, lane * 16 + (i & 3) * 1024, wb + (i >> 2) * 4096);
      __builtin_amdgcn_sched_barrier(0);
      f32x4 acc[7];
#pragma unroll
      for(int n = 0; n < 7; ++n) acc[n] = f32x4{0.f, 0.f, 0.f, 0.f};
      float s1 = 0.f, s2 = 0.f;
#pragma unroll
      for(int ks = 0; ks < 14; ++ks) {
#pragma unroll
        for(int n = 0; n < 7; ++n) acc[n] = mfma32(*(LAS v4u *)(L + L_X + (ks * 7 + n) * 1024 + lane * 16), xf[ks], acc[n]);
#pragma unroll
        for(int j = 0; j < 4; ++j) s1 = dot2(xf[ks][j], 0x3F803F80u, s1), s2 = dot2(xf[ks][j], xf[ks][j], s2);
      }
      s1 = gsum(s1), s2 = gsum(s2);
      const float mu = s1 * (1.f / D), var = fmaxf(s2 * (1.f / D) - mu * mu, 0.f), r1 = __builtin_amdgcn_rsqf(var + EPS);
      const float rs = r1 * __builtin_amdgcn_rsqf(var * r1 * r1 + EPS);
      unsigned hp[14];
#pragma unroll
      for(int n = 0; n < 7; ++n) {
        const f32x4 c = *(LAS f32x4 *)(L + LC_CS1 + n * 64 + g * 16), bb = *(LAS f32x4 *)(L + LC_B1 + n * 64 + g * 16);
        float hv[4];
#pragma unroll
        for(int r = 0; r < 4; ++r) {
          const float v = (acc[n][r] - mu * c[r]) * rs + bb[r];
          const float e = __builtin_amdgcn_exp2f(v * (-2.3022081986f - 0.1029432396f * v * v));
          hv[r] = v * __builtin_amdgcn_rcpf(1.f + e);
        }
        hp[2 * n] = pk2(hv[0], hv[1]), hp[2 * n + 1] = pk2(hv[2], hv[3]);
      }
      const int ho = OFF_H + layer * H_L + tt * 57344;
#pragma unroll
      for(int p = 0; p < 3; ++p) st16(v4u{hp[4 * p], hp[4 * p + 1], hp[4 * p + 2], hp[4 * p + 3]}, rws, lane * 16 + p * 1024, ho + slot * 3072);
      __builtin_amdgcn_raw_buffer_store_b64(v2u{hp[12], hp[13]}, rws, lane * 16, ho + (48 + (slot >> 1)) * 1024 + (slot & 1) * 8, AUX_SC1);
      DRAIN();
      if(lane == 0) set_flag(ws, fl + FL_H + tt * 128 + slot * 8, tag);
    }
    STAMP(12 + layer * 16);
    if(((b0 >> 3) & 15) < 14) {
      IDS();
      const int csl = slot % 7, chalf = slot / 7;
      const int tt0 = 8 * t + 4 * chalf;
      wait_flags(ws, fl + FL_H + tt0 * 128 + lane * 8, tag);
      STAMP(13 + layer * 16);
      const int ho = opq(OFF_H + layer * H_L + tt0 * 57344 + w * 7168);
      v4u hf[12];
#pragma unroll
      for(int i = 0; i < 12; ++i) hf[i] = ld16(rws, lane * 16 + (i >> 2) * 1024, ho + (i & 3) * 57344);
      const int ott = tt0 + (w >> 1), oks = 2 * csl + (w & 1);
      v4u xr, bb;
      __builtin_amdgcn_sched_barrier(0);
      f32x4 acc[4][4];
#pragma unroll
      for(int q = 0; q < 4; ++q)
#pragma unroll
        for(int n = 0; n < 4; ++n) acc[q][n] = f32x4{0.f, 0.f, 0.f, 0.f};
#pragma unroll
      for(int k = 0; k < 7; ++k) {
#pragma unroll
        for(int q = 0; q < 4; ++q) {
          const v4u cur = hf[(k % 3) * 4 + q];
          if(k + 3 < 7) hf[(k % 3) * 4 + q] = ld16(rws, lane * 16 + (k & 3) * 1024, ho + q * 57344 + 3072);
          if(k == 4 && q == 1) xr = ld16(rws, lane * 16, opq(OFF_X1 + layer * X_L + (ott * 14 + oks) * 1024));
          if(k == 4 && q == 2) bb = __builtin_amdgcn_raw_buffer_load_b128(__builtin_amdgcn_make_buffer_rsrc((void *)b2, 0, D * 2, 0x00020000), g * 16, opq(oks * 64), 0);
#pragma unroll
          for(int n = 0; n < 4; ++n) acc[q][n] = mfma32(w2f[k * 4 + n], cur, acc[q][n]);
          __builtin_amdgcn_sched_barrier(0);
        }
      }
      STAMP(14 + layer * 16);
      BARRIER();
#pragma unroll
      for(int q = 0; q < 4; ++q)
#pragma unroll
        for(int n = 0; n < 4; ++n) *(LAS f32x4 *)(L + (((q * 2 + (n >> 1)) * 8 + w) * 2 + (n & 1)) * 1024 + lane * 16) = acc[q][n];
      BARRIER();
      f32x4 a0 = {bflo(xr.x) + bflo(bb.x), bfhi(xr.x) + bfhi(bb.x), bflo(xr.y) + bflo(bb.y), bfhi(xr.y) + bfhi(bb.y)};
      f32x4 a1 = {bflo(xr.z) + bflo(bb.z), bfhi(xr.z) + bfhi(bb.z), bflo(xr.w) + bflo(bb.w), bfhi(xr.w) + bfhi(bb.w)};
#pragma unroll
      for(int s = 0; s < 8; ++s) {
        a0 += *(LAS f32x4 *)(L + ((w * 8 + s) * 2) * 1024 + lane * 16);
        a1 += *(LAS f32x4 *)(L + ((w * 8 + s) * 2 + 1) * 1024 + lane * 16);
      }
      const v4u y = {pk2(a0[0], a0[1]), pk2(a0[2], a0[3]), pk2(a1[0], a1[1]), pk2(a1[2], a1[3])};
      if(layer == 0) {
        st16(y, rws, lane * 16, OFF_X2 + (ott * 14 + oks) * 1024);
        DRAIN();
        if(lane == 0) set_flag(ws, OFF_FLAGS + FL_X2 + ott * 128 + oks * 8, tag);
      } else {
        *(GAS v4u *)(out + (16 * ott + li) * D + 32 * oks + 8 * g) = y;
      }
    }
    STAMP(15 + layer * 16);
    if(layer == 0) {
      IDS();
      BARRIER();
      for(int i = w; i < 84; i += 8) dma1k(rws, lane * 16, OFF_WT + (ID_WQ + slot * 84 + i) * 1024, ldsb + L_X + i * 1024);
    }
  }
  STAMP(35);
}
}

extern "C" void kernel_launch(void *const *d_in, const int *in_sizes, int n_in, void *d_out, int out_size, void *d_ws, size_t ws_size,
    hipStream_t stream) {
  fused_kernel<<<NWG, NTHR, 0, stream>>>((const u16 *)d_in[0], (const u16 *)d_in[1], (const u16 *)d_in[2], (const u16 *)d_in[4],
      (const u16 *)d_in[5], (const u16 *)d_in[6], (const u16 *)d_in[7], (u16 *)d_out, (unsigned char *)d_ws);
}
```

```cpp
#include <hip/hip_runtime.h>
#include <stdint.h>

#define GAS __attribute__((address_space(1)))
#define LAS __attribute__((address_space(3)))
#define DI __device__ __forceinline__
typedef unsigned v4u __attribute__((ext_vector_type(4)));
typedef unsigned v2u __attribute__((ext_vector_type(2)));
typedef float f32x4 __attribute__((ext_vector_type(4)));
typedef float f32x2 __attribute__((ext_vector_type(2)));
typedef __bf16 bf16x8 __attribute__((ext_vector_type(8)));
typedef __bf16 bf16x2 __attribute__((ext_vector_type(2)));
typedef short s16x4 __attribute__((ext_vector_type(4)));
typedef unsigned long long u64;
typedef unsigned short u16;
typedef __amdgpu_buffer_rsrc_t rsrc_t;

#define MAPQ
#define XRING 8
#define PREP_FIRST 1
#define PF_KV 35
#define PF_T 63
#define PF_O 98
#define PF_WQ 56
#define W2_A 8
#define W2_S 2
#define W2_B 24
#define CSL(s) ((s) >= 14 ? 0 : (s) >= 7 ? (s)-7 : (s))
#define TILE_OF(b) ((((b)&7) >> 1) * 4 + ((b) >> 6))
#define SLOT_OF(b) (((b)&1) * 8 + (((b) >> 3) & 7))
namespace {
constexpr int S = 2048, D = 448, F = 1792;
constexpr int NWG = 256, NTHR = 512;
constexpr float EPS = 1e-5f;
constexpr int AUX_SC1 = 16;
constexpr unsigned SPIN_CAP = 1u << 16;
constexpr int OOB = 0x7F000000;

constexpr int OFF_EPOCH = 0;
constexpr int OFF_PREPF = 1024;
constexpr int OFF_FLAGS = 8192, FLAGS_L = 81920;
constexpr int FL_KV = 0, FL_O = 4096, FL_X1 = 8192, FL_H = 12288, FL_X2 = 16384;
constexpr int OFF_WT = 262144;
constexpr int ID_WO = 0, ID_W1 = 448, ID_W2 = 2016, ID_WQ = 3584, ID_END = 4928;
constexpr int OFF_CSP = OFF_WT + ID_END * 1024;
constexpr int OFF_ST = OFF_CSP + ID_END * 64 + 3072;
constexpr int ST_L = 128 * 16 * 128;
constexpr int OFF_KV = OFF_ST + 3 * ST_L;
constexpr int KV_L = 16 * 128 * 2048;
constexpr int OFF_O = OFF_KV + 2 * KV_L;
constexpr int O_L = 128 * 16 * 1024;
constexpr int OFF_X1 = OFF_O + 2 * O_L;
constexpr int X_L = 128 * 14 * 1024;
constexpr int OFF_H = OFF_X1 + 2 * X_L;
constexpr int H_L = 128 * 56 * 1024;
constexpr int OFF_X2 = OFF_H + 2 * H_L;
constexpr int OFF_STAMP = OFF_X2 + X_L;
constexpr int WS_BYTES = OFF_STAMP + 256 * 64 * 8;
static_assert(OFF_KV % 1024 == 0 && OFF_ST % 1024 == 0, "align");

constexpr int L_X = 0;
constexpr int L_KV = 100352 - 32768;
constexpr int L_WO = 100352 + 16384;
constexpr int L_RED = L_WO - 57344;
constexpr int L_C = L_WO + 32768;
constexpr int LC_CSQ = L_C, LC_CS1 = L_C + 512, LC_B1 = L_C + 1024;
constexpr int LDS_BYTES = L_C + 2048;
constexpr int WQ_EARLY = L_RED / 1024;
constexpr int W1_EARLY = L_KV / 1024;
static_assert(PF_T <= W1_EARLY && PF_WQ <= WQ_EARLY, "prefetch ranges");

DI f32x4 mfma32(v4u a, v4u b, f32x4 c) {
  return __builtin_amdgcn_mfma_f32_16x16x32_bf16(__builtin_bit_cast(bf16x8, a), __builtin_bit_cast(bf16x8, b), c, 0, 0, 0);
}
DI f32x4 mfma16(v2u a, v2u b, f32x4 c) {
  return __builtin_amdgcn_mfma_f32_16x16x16bf16_1k(__builtin_bit_cast(s16x4, a), __builtin_bit_cast(s16x4, b), c, 0, 0, 0);
}
DI unsigned pk2(float a, float b) {
  f32x2 v = {a, b};
  return __builtin_bit_cast(unsigned, __builtin_convertvector(v, bf16x2));
}
DI float bflo(unsigned u) { return __uint_as_float(u << 16); }
DI float bfhi(unsigned u) { return __uint_as_float(u & 0xFFFF0000u); }
DI float dot2(unsigned a, unsigned b, float c) {
  return __builtin_amdgcn_fdot2_f32_bf16(__builtin_bit_cast(bf16x2, a), __builtin_bit_cast(bf16x2, b), c, false);
}
DI float gsum(float x) {
  auto r = __builtin_amdgcn_permlane32_swap(__float_as_uint(x), __float_as_uint(x), false, false);
  x = __uint_as_float(r[0]) + __uint_as_float(r[1]);
  auto q = __builtin_amdgcn_permlane16_swap(__float_as_uint(x), __float_as_uint(x), false, false);
  return __uint_as_float(q[0]) + __uint_as_float(q[1]);
}
DI float gmax(float x) {
  auto r = __builtin_amdgcn_permlane32_swap(__float_as_uint(x), __float_as_uint(x), false, false);
  x = fmaxf(fmaxf(x, __uint_as_float(r[0])), __uint_as_float(r[1]));
  auto q = __builtin_amdgcn_permlane16_swap(__float_as_uint(x), __float_as_uint(x), false, false);
  return fmaxf(fmaxf(x, __uint_as_float(q[0])), __uint_as_float(q[1]));
}
DI v4u ld16(rsrc_t rs, int voff, int soff) { return __builtin_amdgcn_raw_buffer_load_b128(rs, voff, soff, AUX_SC1); }
DI v2u ld8(rsrc_t rs, int voff, int soff) { return __builtin_amdgcn_raw_buffer_load_b64(rs, voff, soff, AUX_SC1); }
DI void st16(v4u v, rsrc_t rs, int voff, int soff) { __builtin_amdgcn_raw_buffer_store_b128(v, rs, voff, soff, AUX_SC1); }
DI void st8(v2u v, rsrc_t rs, int voff, int soff) { __builtin_amdgcn_raw_buffer_store_b64(v, rs, voff, soff, AUX_SC1); }
DI int opq(int v) {
  asm volatile("" : "+s"(v));
  return v;
}
DI int opqv(int v) {
  asm volatile("" : "+v"(v));
  return v;
}
DI v2u trread(LAS unsigned char *p) { return __builtin_bit_cast(v2u, __builtin_amdgcn_ds_read_tr16_b64_v4i16((LAS s16x4 *)p)); }
#define DRAIN() asm volatile("s_waitcnt vmcnt(0)" ::: "memory")
#define BARRIER() asm volatile("s_waitcnt lgkmcnt(0)\n\ts_barrier" ::: "memory")
#define SB() __builtin_amdgcn_sched_barrier(0)
DI void dma1k_plain(rsrc_t rs, int voff, int soff, unsigned ldsaddr) {
  asm volatile("s_mov_b32 m0, %0\n\ts_nop 0\n\tbuffer_load_dwordx4 %1, %2, %3 offen lds" ::"s"(ldsaddr), "v"(voff), "s"(rs), "s"(soff) : "memory");
}
#define dma1k(rs, voff, soff, ldsaddr) \
  asm volatile("s_mov_b32 m0, %0\n\ts_nop 0\n\tbuffer_load_dwordx4 %1, %2, %3 offen sc1 lds" ::"s"((unsigned)(ldsaddr)), "v"((int)(voff)), "s"(rs), "s"((int)(soff)) : "memory")
DI void wait_flags(const unsigned char *ws, int off, u64 tag) {
  const GAS u64 *p = (const GAS u64 *)(ws + off);
  for(unsigned spin = 0; spin < SPIN_CAP; ++spin) {
    const u64 v = __hip_atomic_load(p, __ATOMIC_RELAXED, __HIP_MEMORY_SCOPE_AGENT);
    if(__all(v == tag)) break;
    __builtin_amdgcn_s_sleep(1);
  }
  __builtin_amdgcn_fence(__ATOMIC_ACQUIRE, "wavefront");
}
DI void set_flag(unsigned char *ws, int off, u64 tag) {
  __hip_atomic_store((GAS u64 *)(ws + off), tag, __ATOMIC_RELAXED, __HIP_MEMORY_SCOPE_AGENT);
}
DI v2u piece_stats(v4u y) {
  float s1 = 0.f, s2 = 0.f;
#pragma unroll
  for(int j = 0; j < 4; ++j) s1 = dot2(y[j], 0x3F803F80u, s1), s2 = dot2(y[j], y[j], s2);
  return v2u{__float_as_uint(gsum(s1)), __float_as_uint(gsum(s2))};
}

#define STAMP(i)

__global__ void __launch_bounds__(NTHR, 2)
    fused_kernel(const u16 *__restrict__ x, const u16 *__restrict__ wqkv, const u16 *__restrict__ wo, const u16 *__restrict__ w1,
        const u16 *__restrict__ b1, const u16 *__restrict__ w2, const u16 *__restrict__ b2, u16 *__restrict__ out, unsigned char *ws) {
  __shared__ __attribute__((aligned(16))) unsigned char smem[LDS_BYTES];
  LAS unsigned char *L = (LAS unsigned char *)smem;
  const unsigned ldsb = (unsigned)(size_t)L;
  const int tid = threadIdx.x, w0 = __builtin_amdgcn_readfirstlane(tid >> 6), b0 = blockIdx.x;
#define IDS() \
  const int ti = opqv(tid), lane = ti & 63, w = opq(w0), b = opq(b0), t = TILE_OF(b), slot = SLOT_OF(b); \
  const int tt = 8 * t + w, g = lane >> 4, li = lane & 15; \
  (void)g, (void)li, (void)tt, (void)slot
#define WT_DMA(id0, from, to, w_, nw_, ldsoff) \
  for(int i_ = (from) + (w_); i_ < (to); i_ += (nw_)) dma1k(rws, lane * 16, OFF_WT + ((id0) + i_) * 1024, ldsb + (ldsoff) + i_ * 1024)
#define W2_LOAD(from, to) \
  { \
    const int wb = opq(OFF_WT + (ID_W2 + CSL(slot) * 224 + w * 28) * 1024); \
    _Pragma("unroll") for(int i = (from); i < (to); ++i) w2f[i] = ld16(rws, lane * 16 + (i & 3) * 1024, wb + (i >> 2) * 4096); \
    SB(); \
  }
#define WQ_RAW_DMA() \
  { \
    const rsrc_t rq = __builtin_amdgcn_make_buffer_rsrc((void *)wqkv, 0, D * 3 * D * 2, 0x00020000); \
    const int vq = (lane >> 2) * 2688 + (lane & 3) * 16 + 56 * slot - 8 * ((slot ^ (lane >> 5)) & 1); \
    for(int blk = w; blk < 42; blk += 8) { \
      const int ks = blk / 3, so = ks * 32 * 2688 + (blk - ks * 3) * 896; \
      dma1k_plain(rq, vq, so, ldsb + L_X + blk * 2048); \
      dma1k_plain(rq, vq, so + 16 * 2688, ldsb + L_X + blk * 2048 + 1024); \
    } \
  }
#define ROW_STATS(stoff) \
  _Pragma("unroll") for(int i = 0; i < 4; ++i) sp[i] = ld8(rws, (i == 3 && g > 1) ? OOB : li * 8 + g * 128 + i * 512, (stoff) + tt * 2048)
#define SUM_STATS() \
  const float s1 = gsum((__uint_as_float(sp[0].x) + __uint_as_float(sp[1].x)) + (__uint_as_float(sp[2].x) + __uint_as_float(sp[3].x))); \
  const float s2 = gsum((__uint_as_float(sp[0].y) + __uint_as_float(sp[1].y)) + (__uint_as_float(sp[2].y) + __uint_as_float(sp[3].y)))
  const rsrc_t rws = __builtin_amdgcn_make_buffer_rsrc((void *)ws, 0, WS_BYTES, 0x00020000);
  STAMP(0);
  u64 tag = 0;
  v4u w2f[28];

#pragma unroll
  for(int layer = 0; layer < 2; ++layer) {
    const int fl = OFF_FLAGS + layer * FLAGS_L;
    v4u qf;
    {
      IDS();
      v4u xf[14];
      float mu = 0.f, rs = 1.f, mur[4], rsr[4];
      f32x4 acc[6];
#pragma unroll
      for(int n = 0; n < 6; ++n) acc[n] = f32x4{0.f, 0.f, 0.f, 0.f};
      v4u wc[6], wn[6];
#define RDWQ(ks, n) wn[n] = *(LAS v4u *)(L + L_X + ((ks)*6 + (n)) * 1024 + lane * 16)
#define QKV_GEMM(RING) \
  _Pragma("unroll") for(int ks = 0; ks < 14; ++ks) { \
    _Pragma("unroll") for(int n = 0; n < 6; ++n) wc[n] = wn[n]; \
    if(RING && ks + XRING < 14) xf[ks + XRING] = ld16(rws, lane * 16 + ((ks + XRING) & 3) * 1024, xb + ((ks + XRING) >> 2) * 4096); \
    _Pragma("unroll") for(int n = 0; n < 6; ++n) { \
      if(ks < 13) RDWQ(ks + 1, n); \
      acc[n] = n < 4 ? mfma32(wc[n], xf[ks], acc[n]) : mfma32(xf[ks], wc[n], acc[n]); \
      __builtin_amdgcn_sched_group_barrier(0x100, 1, 0); \
      __builtin_amdgcn_sched_group_barrier(0x8, 1, 0); \
    } \
    SB(); \
  }
      const int xb = opq(OFF_X2 + tt * 14336);
      if(layer == 0) {
        v2u ep;
        asm volatile("buffer_load_dwordx2 %0, off, %1, 0 sc1" : "=v"(ep) : "s"(rws) : "memory");
        {
          const rsrc_t rx = __builtin_amdgcn_make_buffer_rsrc((void *)x, 0, S * D * 2, 0x00020000);
          const int xvo = (16 * tt + li) * (D * 2) + g * 16;
#pragma unroll
          for(int ks = 0; ks < 14; ++ks)
            asm volatile("buffer_load_dwordx4 %0, %1, %2, 0 offen offset:%3" : "=v"(xf[ks]) : "v"(xvo), "s"(rx), "i"(ks * 64) : "memory");
        }
        unsigned pf[3][8];
#pragma unroll
        for(int rd = 0; rd < 3; ++rd) {
          const int id = rd * (NWG * 8) + b * 8 + w;
          const u16 *src;
          int bytes, ld, rowA, rowB, col;
          bool okA = true, okB = true;
          const int pc = 8 * (li >> 2) + (li & 3);
          if(id < ID_W1) {
            const int sl = id >> 5, ks = (id & 31) >> 1, e = id & 1;
            src = wo, bytes = D * D * 2, ld = D, rowA = 28 * ks + 8 * g, rowB = rowA + 4;
            col = 32 * sl + pc + 4 * e;
            okB = g < 3;
          } else if(id < ID_W2) {
            const int i2 = id - ID_W1, sl = i2 / 98, rem = i2 % 98, ks = rem / 7, nt = rem % 7;
            src = w1, bytes = D * F * 2, ld = F, rowA = 32 * ks + 8 * g, rowB = rowA + 4;
            col = nt < 6 ? 112 * sl + 32 * (nt >> 1) + pc + 4 * (nt & 1) : 112 * sl + 96 + li;
          } else if(id < ID_WQ) {
            const int i2 = id - ID_W2, cs = i2 / 224, rem = i2 % 224, hks = rem >> 2, nt = rem & 3;
            src = w2, bytes = F * D * 2, ld = D;
            if(hks < 48) rowA = 112 * (hks / 3) + 32 * (hks % 3) + 8 * g, rowB = rowA + 4;
            else rowA = 224 * (hks - 48) + 96 + 4 * g, rowB = rowA + 112;
            col = 64 * cs + 32 * (nt >> 1) + pc + 4 * (nt & 1);
          } else {
            const int i2 = id - ID_WQ, head = i2 / 84, rem = i2 % 84, ks = rem / 6, nt = rem % 6, dc = pc + 4 * (nt & 1);
            src = wqkv, bytes = D * 3 * D * 2, ld = 3 * D, rowA = 32 * ks + 8 * g, rowB = rowA + 4;
            col = (nt >> 1) * D + 28 * head + dc;
            okA = okB = dc < 28 && id < ID_END;
          }
          const rsrc_t rsw = __builtin_amdgcn_make_buffer_rsrc((void *)src, 0, bytes, 0x00020000);
          const int oa = okA ? (rowA * ld + col) * 2 : OOB, ob = okB ? (rowB * ld + col) * 2 : OOB;
          asm volatile(
              "s_nop 4\n\t"
              "buffer_load_ushort %0, %8, %10, 0 offen\n\tbuffer_load_ushort %1, %8, %10, %11 offen\n\t"
              "buffer_load_ushort %2, %8, %10, %12 offen\n\tbuffer_load_ushort %3, %8, %10, %13 offen\n\t"
              "buffer_load_ushort %4, %9, %10, 0 offen\n\tbuffer_load_ushort %5, %9, %10, %11 offen\n\t"
              "buffer_load_ushort %6, %9, %10, %12 offen\n\tbuffer_load_ushort %7, %9, %10, %13 offen"
              : "=&v"(pf[rd][0]), "=&v"(pf[rd][1]), "=&v"(pf[rd][2]), "=&v"(pf[rd][3]), "=&v"(pf[rd][4]), "=&v"(pf[rd][5]), "=&v"(pf[rd][6]),
              "=&v"(pf[rd][7])
              : "v"(oa), "v"(ob), "s"(rsw), "s"(ld * 2), "s"(ld * 4), "s"(ld * 6)
              : "memory");
        }
        WQ_RAW_DMA();
        asm volatile("s_waitcnt vmcnt(34)"
                     : "+v"(xf[0]), "+v"(xf[1]), "+v"(xf[2]), "+v"(xf[3]), "+v"(xf[4]), "+v"(xf[5]), "+v"(xf[6]), "+v"(xf[7]), "+v"(xf[8]),
                     "+v"(xf[9]), "+v"(xf[10]), "+v"(xf[11]), "+v"(xf[12]), "+v"(xf[13]), "+v"(ep)::"memory");
        STAMP(1);
        tag = (((u64)(unsigned)__builtin_amdgcn_readfirstlane(ep.y) << 32) | (unsigned)__builtin_amdgcn_readfirstlane(ep.x)) + 1ull;
        float s1 = 0.f, s2 = 0.f, s3 = 0.f, s4 = 0.f;
#pragma unroll
        for(int ks = 0; ks < 14; ++ks) {
          s1 = dot2(xf[ks][0], 0x3F803F80u, s1), s2 = dot2(xf[ks][0], xf[ks][0], s2);
          s3 = dot2(xf[ks][1], 0x3F803F80u, s3), s4 = dot2(xf[ks][1], xf[ks][1], s4);
          s1 = dot2(xf[ks][2], 0x3F803F80u, s1), s2 = dot2(xf[ks][2], xf[ks][2], s2);
          s3 = dot2(xf[ks][3], 0x3F803F80u, s3), s4 = dot2(xf[ks][3], xf[ks][3], s4);
        }
        s1 = gsum(s1 + s3), s2 = gsum(s2 + s4);
        const float m0 = s1 * (1.f / D), r0 = __builtin_amdgcn_rsqf(fmaxf(s2 * (1.f / D) - m0 * m0, 0.f) + EPS), nm = -m0 * r0;
#pragma unroll
        for(int ks = 0; ks < 14; ++ks)
#pragma unroll
          for(int j = 0; j < 4; ++j) xf[ks][j] = pk2(bflo(xf[ks][j]) * r0 + nm, bfhi(xf[ks][j]) * r0 + nm);
        if(ti < 96) *(LAS float *)(L + LC_CSQ + ti * 4) = 0.f;
        STAMP(2);
        asm volatile("s_waitcnt vmcnt(%24)"
                     : "+v"(pf[0][0]), "+v"(pf[0][1]), "+v"(pf[0][2]), "+v"(pf[0][3]), "+v"(pf[0][4]), "+v"(pf[0][5]), "+v"(pf[0][6]),
                     "+v"(pf[0][7]), "+v"(pf[1][0]), "+v"(pf[1][1]), "+v"(pf[1][2]), "+v"(pf[1][3]), "+v"(pf[1][4]), "+v"(pf[1][5]),
                     "+v"(pf[1][6]), "+v"(pf[1][7]), "+v"(pf[2][0]), "+v"(pf[2][1]), "+v"(pf[2][2]), "+v"(pf[2][3]), "+v"(pf[2][4]),
                     "+v"(pf[2][5]), "+v"(pf[2][6]), "+v"(pf[2][7])
                     : "i"(PREP_FIRST ? 10 : 0)
                     : "memory");
#pragma unroll
        for(int rd = 0; rd < 3; ++rd) {
          const int id = rd * (NWG * 8) + b * 8 + w;
          const unsigned *e = pf[rd];
          const v4u f = {e[0] | (e[1] << 16), e[2] | (e[3] << 16), e[4] | (e[5] << 16), e[6] | (e[7] << 16)};
          st16(f, rws, id < ID_END ? OFF_WT + lane * 16 : OOB, id * 1024);
          float cs = 0.f;
          cs = dot2(f.x, 0x3F803F80u, cs), cs = dot2(f.y, 0x3F803F80u, cs), cs = dot2(f.z, 0x3F803F80u, cs), cs = dot2(f.w, 0x3F803F80u, cs);
          cs = gsum(cs);
          __builtin_amdgcn_raw_buffer_store_b32(__float_as_uint(cs), rws, (lane < 16 && id < ID_END) ? OFF_CSP + lane * 4 : OOB, id * 64, AUX_SC1);
        }
        STAMP(25);
        asm volatile("s_waitcnt vmcnt(6)" ::: "memory");
        STAMP(24);
        LAS unsigned char *wq = L + L_X + (8 * g + (li >> 2)) * 64 + (li & 3) * 16 + 8 * ((slot ^ g) & 1);
        for(int blk = w; blk < 42; blk += 8) {
          const v2u a0 = trread(wq + blk * 2048), a1 = trread(wq + blk * 2048 + 256), c0 = trread(wq + blk * 2048 + 8),
                    c1 = trread(wq + blk * 2048 + 264);
          const v4u f0 = {a0.x, a0.y, a1.x, a1.y}, f1 = {c0.x, c0.y, c1.x, c1.y};
          asm volatile("s_waitcnt lgkmcnt(0)" ::"v"(f0), "v"(f1) : "memory");
          *(LAS v4u *)(L + L_X + blk * 2048 + lane * 16) = f0;
          *(LAS v4u *)(L + L_X + blk * 2048 + 1024 + lane * 16) = f1;
        }
        DRAIN();
        BARRIER();
        if(ti == 0) set_flag(ws, OFF_PREPF + b * 8, tag);
        STAMP(3);
#pragma unroll
        for(int n = 0; n < 6; ++n) RDWQ(0, n);
#pragma unroll
        for(int r = 0; r < 4; ++r) mur[r] = 0.f, rsr[r] = 1.f;
        SB();
        QKV_GEMM(false);
      } else {
        STAMP(31);
        v2u sp[4];
        ROW_STATS(OFF_ST + 2 * ST_L);
#pragma unroll
        for(int ks = 0; ks < XRING; ++ks) xf[ks] = ld16(rws, lane * 16 + (ks & 3) * 1024, xb + (ks >> 2) * 4096);
        SB();
        asm volatile("s_waitcnt vmcnt(%0)" ::"i"(4 + XRING) : "memory");
        BARRIER();
        STAMP(33);
#pragma unroll
        for(int n = 0; n < 6; ++n) RDWQ(0, n);
        SUM_STATS();
        mu = s1 * (1.f / D), rs = __builtin_amdgcn_rsqf(fmaxf(s2 * (1.f / D) - mu * mu, 0.f) + EPS);
#pragma unroll
        for(int r = 0; r < 4; ++r) mur[r] = __shfl(mu, 4 * g + r), rsr[r] = __shfl(rs, 4 * g + r);
        SB();
        QKV_GEMM(true);
      }
      STAMP(4 + layer * 30);
      const float qs = rs * (0.18898223650461362f * 1.4426950408889634f);
      v4u kf, vf;
      {
        f32x4 c[4];
#pragma unroll
        for(int n = 0; n < 4; ++n) c[n] = *(LAS f32x4 *)(L + LC_CSQ + n * 64 + g * 16);
        const float c4 = *(LAS float *)(L + LC_CSQ + 256 + li * 4), c5 = *(LAS float *)(L + LC_CSQ + 320 + li * 4);
        f32x4 a0 = (acc[2] - mu * c[2]) * rs, a1 = (acc[3] - mu * c[3]) * rs;
        kf = v4u{pk2(a0[0], a0[1]), pk2(a0[2], a0[3]), g == 3 ? 0u : pk2(a1[0], a1[1]), g == 3 ? 0u : pk2(a1[2], a1[3])};
#pragma unroll
        for(int r = 0; r < 4; ++r) a0[r] = (acc[4][r] - mur[r] * c4) * rsr[r], a1[r] = (acc[5][r] - mur[r] * c5) * rsr[r];
        const unsigned vpad = li == 12 ? 0x3F803F80u : 0u;
        vf = v4u{pk2(a0[0], a0[1]), pk2(a0[2], a0[3]), li >= 12 ? vpad : pk2(a1[0], a1[1]), li >= 12 ? vpad : pk2(a1[2], a1[3])};
        const int kvo = OFF_KV + layer * KV_L + (slot * 128 + tt) * 2048;
        st16(kf, rws, lane * 16, kvo);
        st16(vf, rws, lane * 16 + 1024, kvo);
        a0 = (acc[0] - mu * c[0]) * qs, a1 = (acc[1] - mu * c[1]) * qs;
        qf = v4u{pk2(a0[0], a0[1]), pk2(a0[2], a0[3]), g == 3 ? 0x00003F80u : pk2(a1[0], a1[1]), g == 3 ? 0u : pk2(a1[2], a1[3])};
      }
      *(LAS v4u *)(L + L_KV + (16 + w) * 2048 + lane * 16) = kf;
      *(LAS v4u *)(L + L_KV + (16 + w) * 2048 + 1024 + lane * 16) = vf;
      STAMP(5 + layer * 30);
    }
    DRAIN();
    BARRIER();
    {
      IDS();
      if(w == 7 && lane == 0) set_flag(ws, fl + FL_KV + (slot * 16 + t) * 8, tag);
      if(layer == 0) {
        for(unsigned spin = 0; spin < SPIN_CAP; ++spin) {
          const v4u p0 = ld16(rws, OFF_PREPF + lane * 32, 0), p1 = ld16(rws, OFF_PREPF + lane * 32 + 16, 0);
          const unsigned tl = (unsigned)tag, th = (unsigned)(tag >> 32);
          if(__all(p0.x == tl && p0.z == tl && p1.x == tl && p1.z == tl && p0.y == th && p0.w == th && p1.y == th && p1.w == th)) break;
          __builtin_amdgcn_s_sleep(1);
          asm volatile("" ::: "memory");
        }
        __builtin_amdgcn_fence(__ATOMIC_ACQUIRE, "wavefront");
        if(b == 0 && ti == 0) set_flag(ws, OFF_EPOCH, tag);
      }
      STAMP(6 + layer * 30);
      if(w == 7) {
        if(t > 0) wait_flags(ws, fl + FL_KV + (slot * 16 + ((lane & 1) && t > 1 ? t - 2 : t - 1)) * 8, tag);
      } else {
        WT_DMA(ID_W1 + slot * 98, 0, PF_KV, w, 7, L_X);
      }
      BARRIER();
      STAMP(7 + layer * 30);
      {
        const int kvr = OFF_KV + layer * KV_L + (slot * 128 + 8 * t - 16) * 2048;
#pragma unroll
        for(int i = 0; i < 4; ++i) {
          const int pc = i * 8 + w;
          if(16 * t - 32 + pc >= 0) dma1k(rws, lane * 16, kvr + pc * 1024, ldsb + L_KV + pc * 1024);
          else *(LAS v4u *)(L + L_KV + pc * 1024 + lane * 16) = v4u{0u, 0u, (g == 3 && !(pc & 1)) ? 0x0000F149u : 0u, 0u};
        }
      }
      f32x4 c0, c16;
#pragma unroll
      for(int r = 0; r < 4; ++r) c0[r] = (4 * g + r > li) ? 0.f : -1e30f, c16[r] = (4 * g + r <= li) ? 0.f : -1e30f;
      DRAIN();
      BARRIER();
      WT_DMA(ID_W1 + slot * 98, PF_KV, PF_T, w, 8, L_X);
      STAMP(8 + layer * 30);
      LAS unsigned char *kv = L + L_KV + w * 2048 + lane * 16;
      f32x4 s[17];
      {
        v4u kf[17];
#pragma unroll
        for(int i = 0; i < 6; ++i) kf[i] = *(LAS v4u *)(kv + i * 2048);
        SB();
#pragma unroll
        for(int i = 0; i < 17; ++i) {
          if(i + 6 < 17) kf[i + 6] = *(LAS v4u *)(kv + (i + 6) * 2048);
          s[i] = mfma32(kf[i], qf, i == 0 ? c0 : i == 16 ? c16 : f32x4{0.f, 0.f, 0.f, 0.f});
          SB();
        }
      }
      v4u vf[17];
#pragma unroll
      for(int i = 0; i < 4; ++i) vf[i] = *(LAS v4u *)(kv + i * 2048 + 1024);
      float mx[4] = {-1e30f, -1e30f, -1e30f, -1e30f};
#pragma unroll
      for(int i = 0; i < 17; ++i) {
        mx[(2 * i) & 3] = fmaxf(fmaxf(mx[(2 * i) & 3], s[i][0]), s[i][1]);
        mx[(2 * i + 1) & 3] = fmaxf(fmaxf(mx[(2 * i + 1) & 3], s[i][2]), s[i][3]);
      }
      const float m = gmax(fmaxf(fmaxf(fmaxf(mx[0], mx[1]), mx[2]), mx[3]));
      f32x4 o0 = {0.f, 0.f, 0.f, 0.f}, o1 = {0.f, 0.f, 0.f, 0.f};
#pragma unroll
      for(int i = 0; i < 17; ++i) {
        if(i + 4 < 17) vf[i + 4] = *(LAS v4u *)(kv + (i + 4) * 2048 + 1024);
        const f32x4 d = s[i] - m;
        v2u p;
        p.x = pk2(__builtin_amdgcn_exp2f(d[0]), __builtin_amdgcn_exp2f(d[1]));
        p.y = pk2(__builtin_amdgcn_exp2f(d[2]), __builtin_amdgcn_exp2f(d[3]));
        o0 = mfma16(v2u{vf[i].x, vf[i].y}, p, o0);
        o1 = mfma16(v2u{vf[i].z, vf[i].w}, p, o1);
        if(i & 1) SB();
      }
      const float inv = __builtin_amdgcn_rcpf(__shfl(o1[0], 48 + li));
      o0 *= inv, o1 *= inv;
      const v4u of = {pk2(o0[0], o0[1]), pk2(o0[2], o0[3]), pk2(o1[0], o1[1]), pk2(o1[2], o1[3])};
      st16(of, rws, lane * 16, OFF_O + layer * O_L + (tt * 16 + slot) * 1024);
      STAMP(9 + layer * 30);
      DRAIN();
      BARRIER();
      STAMP(10 + layer * 30);
      if(w == 7 && lane == 0) set_flag(ws, fl + FL_O + (t * 16 + slot) * 8, tag);
      STAMP(11 + layer * 30);
    }
    {
      IDS();
      if(w == 7) {
        if(slot < 14) wait_flags(ws, fl + FL_O + (t * 16 + li) * 8, tag);
      } else {
        if(layer == 0 && slot < 14) WT_DMA(ID_WO + slot * 32, 0, 32, w, 7, L_WO);
        WT_DMA(ID_W1 + slot * 98, PF_T, PF_O, w, 7, L_X);
      }
      if(slot < 14) {
        const rsrc_t rx = layer == 0 ? __builtin_amdgcn_make_buffer_rsrc((void *)x, 0, S * D * 2, 0x00020000) : rws;
        const v4u xr = ld16(rx, layer == 0 ? (16 * tt + li) * (D * 2) + g * 16 : lane * 16, layer == 0 ? slot * 64 : OFF_X2 + (tt * 14 + slot) * 1024);
        BARRIER();
        STAMP(12 + layer * 30);
        v4u of[16];
        const int ob = opq(OFF_O + layer * O_L + tt * 16384);
#pragma unroll
        for(int h = 0; h < 8; ++h) of[h] = ld16(rws, lane * 16 + (h & 3) * 1024, ob + (h >> 2) * 4096);
        SB();
        if(layer == 0) {
          asm volatile("s_waitcnt vmcnt(8)" ::: "memory");
          BARRIER();
        }
        v4u wf[32];
#pragma unroll
        for(int i = 0; i < 8; ++i) wf[i] = *(LAS v4u *)(L + L_WO + i * 1024 + lane * 16);
        SB();
        f32x4 a0 = {0.f, 0.f, 0.f, 0.f}, a1 = {0.f, 0.f, 0.f, 0.f};
#pragma unroll
        for(int h = 0; h < 16; ++h) {
          if(h < 8) of[h + 8] = ld16(rws, lane * 16 + (h & 3) * 1024, ob + ((h + 8) >> 2) * 4096);
          if(h < 12) wf[2 * h + 8] = *(LAS v4u *)(L + L_WO + (2 * h + 8) * 1024 + lane * 16), wf[2 * h + 9] = *(LAS v4u *)(L + L_WO + (2 * h + 9) * 1024 + lane * 16);
          a0 = mfma32(wf[2 * h], of[h], a0), a1 = mfma32(wf[2 * h + 1], of[h], a1);
          SB();
        }
        const v4u y = {pk2(a0[0] + bflo(xr.x), a0[1] + bfhi(xr.x)), pk2(a0[2] + bflo(xr.y), a0[3] + bfhi(xr.y)),
            pk2(a1[0] + bflo(xr.z), a1[1] + bfhi(xr.z)), pk2(a1[2] + bflo(xr.w), a1[3] + bfhi(xr.w))};
        st16(y, rws, lane * 16, OFF_X1 + layer * X_L + (tt * 14 + slot) * 1024);
        st8(piece_stats(y), rws, lane < 16 ? lane * 8 : OOB, OFF_ST + layer * ST_L + tt * 2048 + slot * 128);
        STAMP(13 + layer * 30);
        DRAIN();
        BARRIER();
        if(w == 7 && lane == 0) set_flag(ws, fl + FL_X1 + (t * 16 + slot) * 8, tag);
      }
      STAMP(14 + layer * 30);
    }
    {
      IDS();
      const int xb = opq(OFF_X1 + layer * X_L + tt * 14336);
      if(w == 7) wait_flags(ws, fl + FL_X1 + (t * 16 + (lane < 14 ? lane : 0)) * 8, tag);
      else WT_DMA(ID_W1 + slot * 98, PF_O, 98, w, 7, L_X);
      if(layer == 0) {
        W2_LOAD(0, W2_A);
        if(ti < 96 + 112) {
          const bool isq = ti < 96;
          const int c = isq ? ti : ti - 96, nt = c >> 4, i = c & 15;
          const int id0 = isq ? ID_WQ + slot * 84 + nt : ID_W1 + slot * 98 + nt, stp = isq ? 6 : 7;
          float sum = 0.f;
#pragma unroll
          for(int ks = 0; ks < 14; ++ks)
            sum += __uint_as_float(__builtin_amdgcn_raw_buffer_load_b32(rws, OFF_CSP + (id0 + ks * stp) * 64 + i * 4, 0, AUX_SC1));
          *(LAS float *)(L + (isq ? LC_CSQ : LC_CS1) + c * 4) = sum;
          if(!isq) {
            const int col = nt < 6 ? 112 * slot + 32 * (nt >> 1) + 8 * (i >> 2) + (i & 3) + 4 * (nt & 1) : 112 * slot + 96 + i;
            *(LAS float *)(L + LC_B1 + c * 4) = bflo(b1[col]);
          }
        }
      }
      BARRIER();
      STAMP(15 + layer * 30);
      v4u xf[14];
      v2u sp[4];
      ROW_STATS(OFF_ST + layer * ST_L);
#pragma unroll
      for(int ks = 0; ks < XRING; ++ks) xf[ks] = ld16(rws, lane * 16 + (ks & 3) * 1024, xb + (ks >> 2) * 4096);
      SB();
      asm volatile("s_waitcnt vmcnt(%0)" ::"i"(4 + XRING) : "memory");
      BARRIER();
      f32x4 acc[7];
#pragma unroll
      for(int n = 0; n < 7; ++n) acc[n] = f32x4{0.f, 0.f, 0.f, 0.f};
      v4u wc[7], wn[7];
#pragma unroll
      for(int n = 0; n < 7; ++n) wn[n] = *(LAS v4u *)(L + L_X + n * 1024 + lane * 16);
      SUM_STATS();
      const float mu = s1 * (1.f / D), var = fmaxf(s2 * (1.f / D) - mu * mu, 0.f), r1 = __builtin_amdgcn_rsqf(var + EPS);
      const float rs = r1 * __builtin_amdgcn_rsqf(var * r1 * r1 + EPS), nmr = -mu * rs;
      const int wb = opq(OFF_WT + (ID_W2 + CSL(slot) * 224 + w * 28) * 1024);
      SB();
#define W1_GEMM(W2L) \
  _Pragma("unroll") for(int ks = 0; ks < 14; ++ks) { \
    _Pragma("unroll") for(int n = 0; n < 7; ++n) wc[n] = wn[n]; \
    if(ks + XRING < 14) xf[ks + XRING] = ld16(rws, lane * 16 + ((ks + XRING) & 3) * 1024, xb + ((ks + XRING) >> 2) * 4096); \
    else if(W2L) { \
      _Pragma("unroll") for(int i = W2_A + (ks + XRING - 14) * W2_S; i < W2_A + (ks + XRING - 13) * W2_S && i < W2_B; ++i) \
          w2f[i] = ld16(rws, lane * 16 + (i & 3) * 1024, wb + (i >> 2) * 4096); \
    } \
    _Pragma("unroll") for(int n = 0; n < 7; ++n) { \
      if(ks < 13) wn[n] = *(LAS v4u *)(L + L_X + ((ks + 1) * 7 + n) * 1024 + lane * 16); \
      acc[n] = mfma32(wc[n], xf[ks], acc[n]); \
      __builtin_amdgcn_sched_group_barrier(0x100, 1, 0); \
      __builtin_amdgcn_sched_group_barrier(0x8, 1, 0); \
    } \
    SB(); \
  }
      if(layer == 0) {
        W1_GEMM(true);
      } else {
        W1_GEMM(false);
      }
      STAMP(16 + layer * 30);
      const int ho = OFF_H + layer * H_L + tt * 57344;
      f32x4 cc[7], bb[7];
#pragma unroll
      for(int n = 0; n < 7; ++n) cc[n] = *(LAS f32x4 *)(L + LC_CS1 + n * 64 + g * 16), bb[n] = *(LAS f32x4 *)(L + LC_B1 + n * 64 + g * 16);
      SB();
      unsigned hp[14];
#pragma unroll
      for(int n = 0; n < 7; ++n) {
        const f32x4 v = acc[n] * rs + (cc[n] * nmr + bb[n]);
        const f32x4 a = v * (v * v * -0.1029432396f + -2.3022081986f);
        const f32x4 e = {__builtin_amdgcn_exp2f(a[0]), __builtin_amdgcn_exp2f(a[1]), __builtin_amdgcn_exp2f(a[2]), __builtin_amdgcn_exp2f(a[3])};
        const f32x4 d = e + 1.f;
        const f32x4 r = {__builtin_amdgcn_rcpf(d[0]), __builtin_amdgcn_rcpf(d[1]), __builtin_amdgcn_rcpf(d[2]), __builtin_amdgcn_rcpf(d[3])};
        const f32x4 hv = v * r;
        hp[2 * n] = pk2(hv[0], hv[1]), hp[2 * n + 1] = pk2(hv[2], hv[3]);
        if(n & 1) st16(v4u{hp[2 * n - 2], hp[2 * n - 1], hp[2 * n], hp[2 * n + 1]}, rws, lane * 16 + (n >> 1) * 1024, ho + slot * 3072);
      }
      st8(v2u{hp[12], hp[13]}, rws, lane * 16, ho + (48 + (slot >> 1)) * 1024 + (slot & 1) * 8);
      STAMP(17 + layer * 30);
      DRAIN();
      BARRIER();
      STAMP(18 + layer * 30);
      if(w == 7) {
        if(lane == 0) set_flag(ws, fl + FL_H + (t * 16 + slot) * 8, tag);
        if(slot < 14) wait_flags(ws, fl + FL_H + (t * 16 + li) * 8, tag);
      }
      if(layer == 0) {
        W2_LOAD(W2_B, 28);
        if(w < 7) WT_DMA(ID_WQ + slot * 84, 0, PF_WQ, w, 7, L_X);
      }
    }
    if(SLOT_OF(b0) < 14) {
      IDS();
      const int csl = CSL(slot), chalf = slot >= 7;
      const int tt0 = 8 * t + 4 * chalf;
      BARRIER();
      STAMP(19 + layer * 30);
      const int ho = opq(OFF_H + layer * H_L + tt0 * 57344 + w * 7168);
      v4u hf[8];
#pragma unroll
      for(int i = 0; i < 8; ++i) hf[i] = ld16(rws, lane * 16 + (i >> 2) * 1024, ho + (i & 3) * 57344);
      const int ott = tt0 + (w >> 1), oks = 2 * csl + (w & 1);
      v4u xr, bb;
      SB();
      f32x4 acc[4][4];
#pragma unroll
      for(int q = 0; q < 4; ++q)
#pragma unroll
        for(int n = 0; n < 4; ++n) acc[q][n] = f32x4{0.f, 0.f, 0.f, 0.f};
#pragma unroll
      for(int k = 0; k < 7; ++k) {
#pragma unroll
        for(int q = 0; q < 4; ++q) {
          const v4u cur = hf[(k & 1) * 4 + q];
          if(k + 2 < 7) hf[(k & 1) * 4 + q] = ld16(rws, lane * 16 + (k & 3) * 1024, ho + q * 57344 + 2048 + (k >> 2) * 4096);
          if(k == 5 && q == 1) xr = ld16(rws, lane * 16, opq(OFF_X1 + layer * X_L + (ott * 14 + oks) * 1024));
          if(k == 5 && q == 2)
            bb = __builtin_amdgcn_raw_buffer_load_b128(__builtin_amdgcn_make_buffer_rsrc((void *)b2, 0, D * 2, 0x00020000), g * 16, opq(oks * 64), 0);
#pragma unroll
          for(int n = 0; n < 4; ++n) acc[q][n] = mfma32(w2f[k * 4 + n], cur, acc[q][n]);
          SB();
        }
      }
      STAMP(20 + layer * 30);
      f32x4 a0, a1;
#pragma unroll
      for(int f = 0; f < 8; ++f) {
        const f32x4 p0 = acc[f >> 1][2 * (f & 1)], p1 = acc[f >> 1][2 * (f & 1) + 1];
        if(f == w) a0 = p0, a1 = p1;
        else
          *(LAS v4u *)(L + L_RED + (f * 7 + (w < f ? w : w - 1)) * 1024 + lane * 16) =
              v4u{pk2(p0[0], p0[1]), pk2(p0[2], p0[3]), pk2(p1[0], p1[1]), pk2(p1[2], p1[3])};
      }
      BARRIER();
      STAMP(21 + layer * 30);
      a0 += f32x4{bflo(xr.x) + bflo(bb.x), bfhi(xr.x) + bfhi(bb.x), bflo(xr.y) + bflo(bb.y), bfhi(xr.y) + bfhi(bb.y)};
      a1 += f32x4{bflo(xr.z) + bflo(bb.z), bfhi(xr.z) + bfhi(bb.z), bflo(xr.w) + bflo(bb.w), bfhi(xr.w) + bfhi(bb.w)};
#pragma unroll
      for(int s = 0; s < 7; ++s) {
        const v4u p = *(LAS v4u *)(L + L_RED + (w * 7 + s) * 1024 + lane * 16);
        a0 += f32x4{bflo(p.x), bfhi(p.x), bflo(p.y), bfhi(p.y)};
        a1 += f32x4{bflo(p.z), bfhi(p.z), bflo(p.w), bfhi(p.w)};
      }
      const v4u y = {pk2(a0[0], a0[1]), pk2(a0[2], a0[3]), pk2(a1[0], a1[1]), pk2(a1[2], a1[3])};
      if(layer == 0) {
        st16(y, rws, lane * 16, OFF_X2 + (ott * 14 + oks) * 1024);
        st8(piece_stats(y), rws, lane < 16 ? lane * 8 : OOB, OFF_ST + 2 * ST_L + ott * 2048 + oks * 128);
        STAMP(22 + layer * 30);
        DRAIN();
      } else {
        *(GAS v4u *)(out + (16 * ott + li) * D + 32 * oks + 8 * g) = y;
      }
    }
    STAMP(23 + layer * 30);
    if(layer == 0) {
      IDS();
      BARRIER();
      if(w == 7) {
        if(lane == 0 && slot < 14) set_flag(ws, OFF_FLAGS + FL_X2 + (t * 16 + slot) * 8, tag);
        wait_flags(ws, OFF_FLAGS + FL_X2 + (t * 16 + (lane < 14 ? lane : 0)) * 8, tag);
      } else {
        WT_DMA(ID_WQ + slot * 84, PF_WQ, 84, w, 7, L_X);
      }
      BARRIER();
    }
  }
  STAMP(61);
}
}

extern "C" void kernel_launch(void *const *d_in, const int *in_sizes, int n_in, void *d_out, int out_size, void *d_ws, size_t ws_size,
    hipStream_t stream) {
  fused_kernel<<<NWG, NTHR, 0, stream>>>((const u16 *)d_in[0], (const u16 *)d_in[1], (const u16 *)d_in[2], (const u16 *)d_in[4],
      (const u16 *)d_in[5], (const u16 *)d_in[6], (const u16 *)d_in[7], (u16 *)d_out, (unsigned char *)d_ws);
}
```

```cpp
#include <hip/hip_runtime.h>
#include <stdint.h>

#define GAS __attribute__((address_space(1)))
#define LAS __attribute__((address_space(3)))
#define DI __device__ __forceinline__
typedef unsigned v4u __attribute__((ext_vector_type(4)));
typedef unsigned v2u __attribute__((ext_vector_type(2)));
typedef float f32x4 __attribute__((ext_vector_type(4)));
typedef float f32x2 __attribute__((ext_vector_type(2)));
typedef __bf16 bf16x8 __attribute__((ext_vector_type(8)));
typedef __bf16 bf16x2 __attribute__((ext_vector_type(2)));
typedef short s16x4 __attribute__((ext_vector_type(4)));
typedef unsigned long long u64;
typedef unsigned short u16;
typedef __amdgpu_buffer_rsrc_t rsrc_t;

#define MAPQ
#define CODEPF 1
#define TLBPF 0
#define WARMBACK 0
#define POLLP 0
#define A0_LEAD 6
#define ORD 2
#define XRING 8
#define PREP_FIRST 2
#define PF_KV 35
#define PF_T 63
#define PF_O 98
#define PF_WQ 54
#define W2_A 8
#define W2_S 2
#define W2_B 24
#define CSL(s) ((s) >= 14 ? 0 : (s) >= 7 ? (s)-7 : (s))
#define TILE_OF(b) ((((b)&7) >> 1) * 4 + ((b) >> 6))
#define SLOT_OF(b) (((b)&1) * 8 + (((b) >> 3) & 7))
namespace {
template <int V> struct IntC {
  static constexpr int value = V;
};
constexpr int S = 2048, D = 448, F = 1792;
constexpr int NWG = 256, NTHR = 512;
constexpr float EPS = 1e-5f;
constexpr int AUX_SC1 = 16;
constexpr unsigned SPIN_CAP = 1u << 16;
constexpr int OOB = 0x7F000000;

constexpr int OFF_EPOCH = 0;
constexpr int OFF_PREPF = 1024, OFF_PREPG = 4096;
constexpr int OFF_FLAGS = 8192, FLAGS_L = 81920;
constexpr int FL_KV = 0, FL_O = 4096, FL_X1 = 8192, FL_H = 12288, FL_X2 = 16384;
constexpr int OFF_WT = 262144;
constexpr int ID_WO = 0, ID_W1 = 448, ID_W2 = 2016, ID_WQ = 3584, ID_END = 4928;
constexpr int OFF_CSP = OFF_WT + ID_END * 1024;
constexpr int OFF_ST = OFF_CSP + ID_END * 64 + 3072;
constexpr int ST_L = 128 * 16 * 128;
constexpr int OFF_KV = OFF_ST + 3 * ST_L;
constexpr int KV_L = 16 * 128 * 2048;
constexpr int OFF_O = OFF_KV + 2 * KV_L;
constexpr int O_L = 128 * 16 * 1024;
constexpr int OFF_X1 = OFF_O + 2 * O_L;
constexpr int X_L = 128 * 14 * 1024;
constexpr int OFF_H = OFF_X1 + 2 * X_L;
constexpr int H_L = 128 * 56 * 1024;
constexpr int OFF_X2 = OFF_H + 2 * H_L;
constexpr int OFF_STAMP = OFF_X2 + X_L;
constexpr int WS_BYTES = OFF_STAMP + 2 * 256 * 64 * 8;
static_assert(OFF_KV % 1024 == 0 && OFF_ST % 1024 == 0, "align");

constexpr int L_X = 0;
constexpr int L_KV = 100352 - 32768;
constexpr int L_WO = 100352 + 16384;
constexpr int L_RED = L_WO - 57344;
constexpr int L_C = L_WO + 32768;
constexpr int LC_CSQ = L_C, LC_CS1 = L_C + 512, LC_B1 = L_C + 1024;
constexpr int L_DUMMY = L_C + 2048;
constexpr int LS_Q = L_DUMMY + 2048;
constexpr int LDS_BYTES = LS_Q + 64;
constexpr int WQ_EARLY = L_RED / 1024;
constexpr int W1_EARLY = L_KV / 1024;
static_assert(PF_T <= W1_EARLY && PF_WQ <= WQ_EARLY, "prefetch ranges");

DI f32x4 mfma32(v4u a, v4u b, f32x4 c) {
  return __builtin_amdgcn_mfma_f32_16x16x32_bf16(__builtin_bit_cast(bf16x8, a), __builtin_bit_cast(bf16x8, b), c, 0, 0, 0);
}
DI f32x4 mfma16(v2u a, v2u b, f32x4 c) {
  return __builtin_amdgcn_mfma_f32_16x16x16bf16_1k(__builtin_bit_cast(s16x4, a), __builtin_bit_cast(s16x4, b), c, 0, 0, 0);
}
DI unsigned pk2(float a, float b) {
  f32x2 v = {a, b};
  return __builtin_bit_cast(unsigned, __builtin_convertvector(v, bf16x2));
}
DI float bflo(unsigned u) { return __uint_as_float(u << 16); }
DI float bfhi(unsigned u) { return __uint_as_float(u & 0xFFFF0000u); }
DI float dot2(unsigned a, unsigned b, float c) {
  return __builtin_amdgcn_fdot2_f32_bf16(__builtin_bit_cast(bf16x2, a), __builtin_bit_cast(bf16x2, b), c, false);
}
DI float gsum(float x) {
  auto r = __builtin_amdgcn_permlane32_swap(__float_as_uint(x), __float_as_uint(x), false, false);
  x = __uint_as_float(r[0]) + __uint_as_float(r[1]);
  auto q = __builtin_amdgcn_permlane16_swap(__float_as_uint(x), __float_as_uint(x), false, false);
  return __uint_as_float(q[0]) + __uint_as_float(q[1]);
}
DI float gmax(float x) {
  auto r = __builtin_amdgcn_permlane32_swap(__float_as_uint(x), __float_as_uint(x), false, false);
  x = fmaxf(fmaxf(x, __uint_as_float(r[0])), __uint_as_float(r[1]));
  auto q = __builtin_amdgcn_permlane16_swap(__float_as_uint(x), __float_as_uint(x), false, false);
  return fmaxf(fmaxf(x, __uint_as_float(q[0])), __uint_as_float(q[1]));
}
DI v4u ld16(rsrc_t rs, int voff, int soff) { return __builtin_amdgcn_raw_buffer_load_b128(rs, voff, soff, AUX_SC1); }
DI v2u ld8(rsrc_t rs, int voff, int soff) { return __builtin_amdgcn_raw_buffer_load_b64(rs, voff, soff, AUX_SC1); }
DI void st16(v4u v, rsrc_t rs, int voff, int soff) { __builtin_amdgcn_raw_buffer_store_b128(v, rs, voff, soff, AUX_SC1); }
DI void st8(v2u v, rsrc_t rs, int voff, int soff) { __builtin_amdgcn_raw_buffer_store_b64(v, rs, voff, soff, AUX_SC1); }
DI int opq(int v) {
  asm volatile("" : "+s"(v));
  return v;
}
DI int opqv(int v) {
  asm volatile("" : "+v"(v));
  return v;
}
DI v2u trread(LAS unsigned char *p) { return __builtin_bit_cast(v2u, __builtin_amdgcn_ds_read_tr16_b64_v4i16((LAS s16x4 *)p)); }
#define DRAIN() asm volatile("s_waitcnt vmcnt(0)" ::: "memory")
#define BARRIER() asm volatile("s_waitcnt lgkmcnt(0)\n\ts_barrier" ::: "memory")
#define SB() __builtin_amdgcn_sched_barrier(0)
DI void dma1k_plain(rsrc_t rs, int voff, int soff, unsigned ldsaddr) {
  asm volatile("s_mov_b32 m0, %0\n\ts_nop 0\n\tbuffer_load_dwordx4 %1, %2, %3 offen lds" ::"s"(ldsaddr), "v"(voff), "s"(rs), "s"(soff) : "memory");
}
#define dma1k(rs, voff, soff, ldsaddr) \
  asm volatile("s_mov_b32 m0, %0\n\ts_nop 0\n\tbuffer_load_dwordx4 %1, %2, %3 offen sc1 lds" ::"s"((unsigned)(ldsaddr)), "v"((int)(voff)), "s"(rs), "s"((int)(soff)) : "memory")
DI void wait_flags_(const unsigned char *ws, int off, u64 tag, u64 &v0, u64 &v1) {
  const GAS u64 *p = (const GAS u64 *)(ws + off);
  for(unsigned spin = 0; spin < SPIN_CAP; ++spin) {
    const u64 v = __hip_atomic_load(p, __ATOMIC_RELAXED, __HIP_MEMORY_SCOPE_AGENT);
    if(__all(v == tag)) break;
    __builtin_amdgcn_s_sleep(1);
  }
  __builtin_amdgcn_fence(__ATOMIC_ACQUIRE, "wavefront");
}
#define wait_flags(ws, off, tag) wait_flags_(ws, off, tag, pv0, pv1)
DI void set_flag(unsigned char *ws, int off, u64 tag) {
  __hip_atomic_store((GAS u64 *)(ws + off), tag, __ATOMIC_RELAXED, __HIP_MEMORY_SCOPE_AGENT);
}
DI v2u piece_stats(v4u y) {
  float s1 = 0.f, s2 = 0.f;
#pragma unroll
  for(int j = 0; j < 4; ++j) s1 = dot2(y[j], 0x3F803F80u, s1), s2 = dot2(y[j], y[j], s2);
  return v2u{__float_as_uint(gsum(s1)), __float_as_uint(gsum(s2))};
}

#define STAMP(i)

__global__ void __launch_bounds__(NTHR, 2)
    fused_kernel(const u16 *__restrict__ x, const u16 *__restrict__ wqkv, const u16 *__restrict__ wo, const u16 *__restrict__ w1,
        const u16 *__restrict__ b1, const u16 *__restrict__ w2, const u16 *__restrict__ b2, u16 *__restrict__ out, unsigned char *ws) {
  __shared__ __attribute__((aligned(16))) unsigned char smem[LDS_BYTES];
  LAS unsigned char *L = (LAS unsigned char *)smem;
  const unsigned ldsb = (unsigned)(size_t)L;
  const int tid = threadIdx.x, w0 = __builtin_amdgcn_readfirstlane(tid >> 6), b0 = blockIdx.x;
#define IDS() \
  const int ti = opqv(tid), lane = ti & 63, w = opq(w0), b = opq(b0), t = TILE_OF(b), slot = SLOT_OF(b); \
  const int tt = 8 * t + w, g = lane >> 4, li = lane & 15; \
  (void)g, (void)li, (void)tt, (void)slot
#define WT_DMA(id0, from, to, w_, nw_, ldsoff) \
  for(int i_ = (from) + (w_); i_ < (to); i_ += (nw_)) dma1k(rws, lane * 16, OFF_WT + ((id0) + i_) * 1024, ldsb + (ldsoff) + i_ * 1024)
#define W2_LOAD(from, to) \
  { \
    const int wb = opq(OFF_WT + (ID_W2 + CSL(slot) * 224 + w * 28) * 1024); \
    _Pragma("unroll") for(int i = (from); i < (to); ++i) w2f[i] = ld16(rws, lane * 16 + (i & 3) * 1024, wb + (i >> 2) * 4096); \
    SB(); \
  }
#define WARM(n, soff, wstride) \
  if(WARMBACK && w < 7) { \
    _Pragma("unroll") for(int i_ = 0; i_ < (n); ++i_) dma1k(rws, lane * 16, (soff) + i_ * 1024, ldsb + L_DUMMY); \
    if(w == 6) _Pragma("unroll") for(int i_ = 0; i_ < (n); ++i_) dma1k(rws, lane * 16, (soff) + (wstride) + i_ * 1024, ldsb + L_DUMMY); \
  }
#define PREP_WAIT(POFF) \
      { \
        for(unsigned spin = 0; spin < SPIN_CAP; ++spin) { \
          const v4u p0 = ld16(rws, (POFF) + lane * 32, 0), p1 = ld16(rws, (POFF) + lane * 32 + 16, 0); \
          const unsigned tl = (unsigned)tag, th = (unsigned)(tag >> 32); \
          if(__all(p0.x == tl && p0.z == tl && p1.x == tl && p1.z == tl && p0.y == th && p0.w == th && p1.y == th && p1.w == th)) break; \
          __builtin_amdgcn_s_sleep(1); \
          asm volatile("" ::: "memory"); \
        } \
        __builtin_amdgcn_fence(__ATOMIC_ACQUIRE, "wavefront"); \
        if((POFF) == OFF_PREPG && b == 0 && ti == 0) set_flag(ws, OFF_EPOCH, tag); \
      }
#define ROW_STATS(stoff) \
  _Pragma("unroll") for(int i = 0; i < 4; ++i) sp[i] = ld8(rws, (i == 3 && g > 1) ? OOB : li * 8 + g * 128 + i * 512, (stoff) + tt * 2048)
#define SUM_STATS() \
  const float s1 = gsum((__uint_as_float(sp[0].x) + __uint_as_float(sp[1].x)) + (__uint_as_float(sp[2].x) + __uint_as_float(sp[3].x))); \
  const float s2 = gsum((__uint_as_float(sp[0].y) + __uint_as_float(sp[1].y)) + (__uint_as_float(sp[2].y) + __uint_as_float(sp[3].y)))
  const rsrc_t rws = __builtin_amdgcn_make_buffer_rsrc((void *)ws, 0, WS_BYTES, 0x00020000);
  STAMP(0);
  u64 tag = 0, pv0 = 0, pv1 = 0;
  {
    u64 pc;
    int len;
    asm volatile("s_getpc_b64 %0\n.Lfk_here:\n\ts_mov_b32 %1, .Lfk_end-.Lfk_here" : "=s"(pc), "=s"(len));
    if(CODEPF && w0 == 7) {
      const rsrc_t rc = __builtin_amdgcn_make_buffer_rsrc((void *)pc, 0, len, 0x00020000);
      asm volatile("s_nop 4" ::: "memory");
      dma1k_plain(rc, (tid & 63) * 16, (b0 >> 3) * 2048, ldsb + L_DUMMY);
      dma1k_plain(rc, (tid & 63) * 16, (b0 >> 3) * 2048 + 1024, ldsb + L_DUMMY + 1024);
    }
    if(TLBPF && w0 == 6)
      asm volatile("s_mov_b32 m0, %0\n\ts_nop 0\n\tbuffer_load_dword %1, %2, 0 offen sc1 lds" ::"s"(ldsb + L_DUMMY),
                   "v"(((b0 >> 3) * 64 + (tid & 63)) * 65536 + 4096), "s"(rws)
                   : "memory");
  }
  v4u w2f[28];

  auto run_layer = [&](auto LC) __attribute__((always_inline)) {
    constexpr int layer = decltype(LC)::value;
    const int fl = OFF_FLAGS + layer * FLAGS_L;
    v4u qf;
    {
      IDS();
      v4u xf[14];
      float mu, rs, mur[4], rsr[4];
      f32x4 acc[6];
#pragma unroll
      for(int n = 0; n < 6; ++n) acc[n] = f32x4{0.f, 0.f, 0.f, 0.f};
      v4u wc[6], wn[6];
#define RDWQ(ks, n) wn[n] = *(LAS v4u *)(L + L_X + ((ks)*6 + (n)) * 1024 + lane * 16)
#define QKV_GEMM(RING, K0, K1) \
  _Pragma("unroll") for(int n = 0; n < 6; ++n) RDWQ(K0, n); \
  SB(); \
  _Pragma("unroll") for(int ks = K0; ks < K1; ++ks) { \
    _Pragma("unroll") for(int n = 0; n < 6; ++n) wc[n] = wn[n]; \
    if(RING && ks + XRING < 14) xf[ks + XRING] = ld16(rws, lane * 16 + ((ks + XRING) & 3) * 1024, xb + ((ks + XRING) >> 2) * 4096); \
    _Pragma("unroll") for(int n = 0; n < 6; ++n) { \
      if(ks < K1 - 1) RDWQ(ks + 1, n); \
      acc[n] = n < 4 ? mfma32(wc[n], xf[ks], acc[n]) : mfma32(xf[ks], wc[n], acc[n]); \
      __builtin_amdgcn_sched_group_barrier(0x100, 1, 0); \
      __builtin_amdgcn_sched_group_barrier(0x8, 1, 0); \
    } \
    SB(); \
  }
      const int xb = opq(OFF_X2 + tt * 14336);
      if(layer == 0) {
        v2u ep;
        asm volatile("buffer_load_dwordx2 %0, off, %1, 0 sc1" : "=v"(ep) : "s"(rws) : "memory");
        int dst[2], dst1[2], tro[2], tre[2];
        if(ti < 2) *(LAS unsigned *)(L + LS_Q + 4 * ti) = 0u;
        const rsrc_t rx = __builtin_amdgcn_make_buffer_rsrc((void *)x, 0, S * D * 2, 0x00020000);
        const int xvo = (16 * tt + (lane >> 3)) * (D * 2) + (((lane & 7) ^ (lane >> 3)) * 16);
#define X0_DMA() \
  _Pragma("unroll") for(int i = 0; i < 14; ++i) dma1k_plain(rx, xvo, (i & 1) * 8 * (D * 2) + (i >> 1) * 128, ldsb + w * 14336 + i * 1024)
#define PREP_OUT(rd, QPAD) \
  { \
    LAS unsigned char *p = L + L_WO + ((rd)*8 + w) * 2048 + (8 * g + (li >> 2)) * 64 + (li & 3) * tro[rd]; \
    const v2u a0 = trread(p), a1 = trread(p + 256), c0 = trread(p + tre[rd]), c1 = trread(p + tre[rd] + 256); \
    const v4u f0 = {a0.x, a0.y, a1.x, a1.y}; \
    v4u f1 = {c0.x, c0.y, c1.x, c1.y}; \
    if(QPAD && li >= 12) f1 = v4u{0u, 0u, 0u, 0u}; \
    const bool ok = dst[rd] < ID_END; \
    st16(f0, rws, ok ? lane * 16 : OOB, OFF_WT + dst[rd] * 1024); \
    st16(f1, rws, ok ? lane * 16 : OOB, OFF_WT + dst1[rd] * 1024); \
    float ca = 0.f, cb = 0.f; \
    _Pragma("unroll") for(int j = 0; j < 4; ++j) ca = dot2(f0[j], 0x3F803F80u, ca), cb = dot2(f1[j], 0x3F803F80u, cb); \
    ca = gsum(ca), cb = gsum(cb); \
    __builtin_amdgcn_raw_buffer_store_b32( \
        __float_as_uint(g ? cb : ca), rws, (ok && g < 2) ? OFF_CSP + (g ? dst1[rd] : dst[rd]) * 64 + li * 4 : OOB, 0, AUX_SC1); \
  }
        BARRIER();
        if(w < 3) {
          const int id = b * 3 + w, r = lane >> 2, c = lane & 3;
          const int ks = id / 48, rem = id - ks * 48, sec = rem >> 4, head = rem & 15;
          const rsrc_t rq = __builtin_amdgcn_make_buffer_rsrc((void *)wqkv, 0, D * 3 * D * 2, 0x00020000);
          const int o0 = (32 * ks + r) * 2688 + sec * 896 + 56 * head + c * 16;
          dma1k_plain(rq, o0, 0, ldsb + L_WO + w * 2048);
          dma1k_plain(rq, o0, 16 * 2688, ldsb + L_WO + w * 2048 + 1024);
          dst[0] = id < 672 ? ID_WQ + head * 84 + ks * 6 + sec * 2 : ID_END, dst1[0] = dst[0] + 1, tro[0] = 16, tre[0] = 8;
          asm volatile("s_waitcnt vmcnt(0)" : "+v"(ep)::"memory");
          STAMP(25);
          tag = (((u64)(unsigned)__builtin_amdgcn_readfirstlane(ep.y) << 32) | (unsigned)__builtin_amdgcn_readfirstlane(ep.x)) + 1ull;
          PREP_OUT(0, true);
          if(ORD == 2 && lane == 0) __hip_atomic_fetch_add((LAS unsigned *)(L + LS_Q + 4), 1u, __ATOMIC_RELAXED, __HIP_MEMORY_SCOPE_WORKGROUP);
          DRAIN();
          STAMP(26);
          if(lane == 0 && __hip_atomic_fetch_add((LAS unsigned *)(L + LS_Q), 1u, __ATOMIC_RELAXED, __HIP_MEMORY_SCOPE_WORKGROUP) == 2u)
            set_flag(ws, OFF_PREPF + b * 8, tag);
          X0_DMA();
          DRAIN();
        } else {
          if(ORD == 1) X0_DMA();
#pragma unroll
          for(int rd = 0; rd < 2; ++rd) {
            const int id = rd == 0 ? b * 5 + (w - 3) : w < 5 ? 1280 + b * 2 + (w - 3) : 1792, r = lane >> 2, c = lane & 3;
            const u16 *src;
            int bytes, o0, o1;
            tro[rd] = 16, tre[rd] = 8;
            if(id < 224) {
              const int ks = id / 14, sl = id - ks * 14;
              src = wo, bytes = D * D * 2;
              o0 = (28 * ks + r) * 896 + 64 * sl + c * 16, o1 = r < 12 ? o0 + 16 * 896 : OOB;
              dst[rd] = ID_WO + sl * 32 + ks * 2, dst1[rd] = dst[rd] + 1;
            } else if(id < 224 + 672) {
              const int i2 = id - 224, ks = i2 / 48, rem = i2 - ks * 48, sl = rem / 3, p = rem - sl * 3;
              src = w1, bytes = D * F * 2;
              o0 = (32 * ks + r) * 3584 + 224 * sl + 64 * p + c * 16, o1 = o0 + 16 * 3584;
              dst[rd] = ID_W1 + sl * 98 + ks * 7 + 2 * p, dst1[rd] = dst[rd] + 1;
            } else if(id < 896 + 112) {
              const int i2 = id - 896, ks = i2 >> 3, sl = 2 * (i2 & 7);
              src = w1, bytes = D * F * 2;
              o0 = (32 * ks + r) * 3584 + 224 * (sl + (c >> 1)) + 192 + (c & 1) * 16, o1 = o0 + 16 * 3584;
              dst[rd] = ID_W1 + sl * 98 + ks * 7 + 6, dst1[rd] = dst[rd] + 98, tro[rd] = 8, tre[rd] = 32;
            } else {
              const int i2 = id - 1008, hks = i2 / 14, cb = i2 - hks * 14;
              src = w2, bytes = F * D * 2;
              const int row = hks < 48 ? 112 * (hks / 3) + 32 * (hks % 3) + r : 224 * (hks - 48) + 96 + 112 * ((r >> 2) & 1) + 4 * (r >> 3) + (r & 3);
              o0 = row * 896 + 64 * cb + c * 16, o1 = o0 + (hks < 48 ? 16 : 8) * 896;
              dst[rd] = ID_W2 + (cb >> 1) * 224 + hks * 4 + (cb & 1) * 2, dst1[rd] = dst[rd] + 1;
              if(id >= 1792) o0 = o1 = OOB, dst[rd] = dst1[rd] = ID_END;
            }
            const rsrc_t rsw = __builtin_amdgcn_make_buffer_rsrc((void *)src, 0, bytes, 0x00020000);
            asm volatile("s_nop 4" ::: "memory");
            dma1k_plain(rsw, o0, 0, ldsb + L_WO + (rd * 8 + w) * 2048);
            dma1k_plain(rsw, o1, 0, ldsb + L_WO + (rd * 8 + w) * 2048 + 1024);
          }
          if(ORD == 2) {
            for(unsigned spin = 0; spin < (1u << 20); ++spin)
              if(__builtin_amdgcn_readfirstlane(__hip_atomic_load((LAS unsigned *)(L + LS_Q + 4), __ATOMIC_RELAXED, __HIP_MEMORY_SCOPE_WORKGROUP)) >= 3) break;
          }
          if(ORD != 1) X0_DMA();
          STAMP(25);
          asm volatile("s_waitcnt vmcnt(0)" : "+v"(ep)::"memory");
          STAMP(26);
          tag = (((u64)(unsigned)__builtin_amdgcn_readfirstlane(ep.y) << 32) | (unsigned)__builtin_amdgcn_readfirstlane(ep.x)) + 1ull;
          if(w == 7) PREP_WAIT(OFF_PREPF);
          if(ORD == 0) {
            PREP_OUT(0, false);
            PREP_OUT(1, false);
          }
        }
        STAMP(27);
#pragma unroll
        for(int ks = 0; ks < 14; ++ks)
          xf[ks] = *(LAS v4u *)(L + w * 14336 + ((ks >> 1) * 2 + (li >> 3)) * 1024 + (li & 7) * 128 + (((ks & 1) * 4 + g) ^ (li & 7)) * 16);
        BARRIER();
        STAMP(2);
        WT_DMA(ID_WQ + slot * 84, 0, 84, w, 8, L_X);
        float s1 = 0.f, s2 = 0.f, s3 = 0.f, s4 = 0.f;
#pragma unroll
        for(int ks = 0; ks < 14; ++ks) {
          s1 = dot2(xf[ks][0], 0x3F803F80u, s1), s2 = dot2(xf[ks][0], xf[ks][0], s2);
          s3 = dot2(xf[ks][1], 0x3F803F80u, s3), s4 = dot2(xf[ks][1], xf[ks][1], s4);
          s1 = dot2(xf[ks][2], 0x3F803F80u, s1), s2 = dot2(xf[ks][2], xf[ks][2], s2);
          s3 = dot2(xf[ks][3], 0x3F803F80u, s3), s4 = dot2(xf[ks][3], xf[ks][3], s4);
        }
        s1 = gsum(s1 + s3), s2 = gsum(s2 + s4);
        mu = s1 * (1.f / D), rs = __builtin_amdgcn_rsqf(fmaxf(s2 * (1.f / D) - mu * mu, 0.f) + EPS);
#pragma unroll
        for(int r = 0; r < 4; ++r) mur[r] = __shfl(mu, 4 * g + r), rsr[r] = __shfl(rs, 4 * g + r);
        asm volatile("" : "+v"(mu), "+v"(rs), "+v"(mur[0]), "+v"(mur[1]), "+v"(mur[2]), "+v"(mur[3]), "+v"(rsr[0]), "+v"(rsr[1]), "+v"(rsr[2]), "+v"(rsr[3]));
        STAMP(28);
        STAMP(24);
        asm volatile("s_waitcnt vmcnt(5)" ::: "memory");
        BARRIER();
        STAMP(3);
        if(ORD != 0 && w >= 3) {
          PREP_OUT(0, false);
          PREP_OUT(1, false);
        }
        if(ti < 96) {
          float csum = 0.f;
#pragma unroll
          for(int ks = 0; ks < 14; ++ks)
            csum += __uint_as_float(__builtin_amdgcn_raw_buffer_load_b32(rws, OFF_CSP + (ID_WQ + slot * 84 + ks * 6) * 64 + ti * 4, 0, AUX_SC1));
          *(LAS float *)(L + LC_CSQ + ti * 4) = csum;
        }
        QKV_GEMM(false, 0, 7);
        DRAIN();
        BARRIER();
        if(ti == 0) set_flag(ws, OFF_PREPG + b * 8, tag);
        QKV_GEMM(false, 7, 14);
      } else {
        STAMP(31);
        v2u sp[4];
        ROW_STATS(OFF_ST + 2 * ST_L);
#pragma unroll
        for(int ks = 0; ks < XRING; ++ks) xf[ks] = ld16(rws, lane * 16 + (ks & 3) * 1024, xb + (ks >> 2) * 4096);
        SB();
        STAMP(33);
        SUM_STATS();
        mu = s1 * (1.f / D), rs = __builtin_amdgcn_rsqf(fmaxf(s2 * (1.f / D) - mu * mu, 0.f) + EPS);
#pragma unroll
        for(int r = 0; r < 4; ++r) mur[r] = __shfl(mu, 4 * g + r), rsr[r] = __shfl(rs, 4 * g + r);
        QKV_GEMM(true, 0, PF_WQ / 6);
        BARRIER();
        QKV_GEMM(true, PF_WQ / 6, 14);
      }
      STAMP(4 + layer * 30);
      const float qs = rs * (0.18898223650461362f * 1.4426950408889634f);
      v4u kf, vf;
      {
        f32x4 c[4];
#pragma unroll
        for(int n = 0; n < 4; ++n) c[n] = *(LAS f32x4 *)(L + LC_CSQ + n * 64 + g * 16);
        const float c4 = *(LAS float *)(L + LC_CSQ + 256 + li * 4), c5 = *(LAS float *)(L + LC_CSQ + 320 + li * 4);
        f32x4 a0 = (acc[2] - mu * c[2]) * rs, a1 = (acc[3] - mu * c[3]) * rs;
        kf = v4u{pk2(a0[0], a0[1]), pk2(a0[2], a0[3]), g == 3 ? 0u : pk2(a1[0], a1[1]), g == 3 ? 0u : pk2(a1[2], a1[3])};
#pragma unroll
        for(int r = 0; r < 4; ++r) a0[r] = (acc[4][r] - mur[r] * c4) * rsr[r], a1[r] = (acc[5][r] - mur[r] * c5) * rsr[r];
        const unsigned vpad = li == 12 ? 0x3F803F80u : 0u;
        vf = v4u{pk2(a0[0], a0[1]), pk2(a0[2], a0[3]), li >= 12 ? vpad : pk2(a1[0], a1[1]), li >= 12 ? vpad : pk2(a1[2], a1[3])};
        const int kvo = OFF_KV + layer * KV_L + (slot * 128 + tt) * 2048;
        st16(kf, rws, lane * 16, kvo);
        st16(vf, rws, lane * 16 + 1024, kvo);
        a0 = (acc[0] - mu * c[0]) * qs, a1 = (acc[1] - mu * c[1]) * qs;
        qf = v4u{pk2(a0[0], a0[1]), pk2(a0[2], a0[3]), g == 3 ? 0x00003F80u : pk2(a1[0], a1[1]), g == 3 ? 0u : pk2(a1[2], a1[3])};
      }
      *(LAS v4u *)(L + L_KV + (16 + w) * 2048 + lane * 16) = kf;
      *(LAS v4u *)(L + L_KV + (16 + w) * 2048 + 1024 + lane * 16) = vf;
      STAMP(5 + layer * 30);
    }
    DRAIN();
    BARRIER();
    {
      IDS();
      if(w == 7 && lane == 0) set_flag(ws, fl + FL_KV + (slot * 16 + t) * 8, tag);
      WARM(2, OFF_KV + layer * KV_L + (slot * 128 + tt) * 2048, 2048);
      if(layer == 0 && w < 7) PREP_WAIT(OFF_PREPG);
      STAMP(6 + layer * 30);
      if(w == 7) {
        if(t > 0) wait_flags(ws, fl + FL_KV + (slot * 16 + ((lane & 1) && t > 1 ? t - 2 : t - 1)) * 8, tag);
      } else {
        WT_DMA(ID_W1 + slot * 98, 0, PF_KV, w, 7, L_X);
      }
      BARRIER();
      STAMP(7 + layer * 30);
      {
        const int kvr = OFF_KV + layer * KV_L + (slot * 128 + 8 * t - 16) * 2048;
#pragma unroll
        for(int i = 0; i < 4; ++i) {
          const int pc = i * 8 + w;
          if(16 * t - 32 + pc >= 0) dma1k(rws, lane * 16, kvr + pc * 1024, ldsb + L_KV + pc * 1024);
          else *(LAS v4u *)(L + L_KV + pc * 1024 + lane * 16) = v4u{0u, 0u, (g == 3 && !(pc & 1)) ? 0x0000F149u : 0u, 0u};
        }
      }
      f32x4 c0, c16;
#pragma unroll
      for(int r = 0; r < 4; ++r) c0[r] = (4 * g + r > li) ? 0.f : -1e30f, c16[r] = (4 * g + r <= li) ? 0.f : -1e30f;
      DRAIN();
      BARRIER();
      WT_DMA(ID_W1 + slot * 98, PF_KV, PF_T, w, 8, L_X);
      STAMP(8 + layer * 30);
      LAS unsigned char *kv = L + L_KV + w * 2048 + lane * 16;
      f32x4 s[17];
      {
        v4u kf[17];
#pragma unroll
        for(int i = 0; i < 6; ++i) kf[i] = *(LAS v4u *)(kv + i * 2048);
        SB();
#pragma unroll
        for(int i = 0; i < 17; ++i) {
          if(i + 6 < 17) kf[i + 6] = *(LAS v4u *)(kv + (i + 6) * 2048);
          s[i] = mfma32(kf[i], qf, i == 0 ? c0 : i == 16 ? c16 : f32x4{0.f, 0.f, 0.f, 0.f});
          SB();
        }
      }
      v4u vf[17];
#pragma unroll
      for(int i = 0; i < 4; ++i) vf[i] = *(LAS v4u *)(kv + i * 2048 + 1024);
      float mx[4] = {-1e30f, -1e30f, -1e30f, -1e30f};
#pragma unroll
      for(int i = 0; i < 17; ++i) {
        mx[(2 * i) & 3] = fmaxf(fmaxf(mx[(2 * i) & 3], s[i][0]), s[i][1]);
        mx[(2 * i + 1) & 3] = fmaxf(fmaxf(mx[(2 * i + 1) & 3], s[i][2]), s[i][3]);
      }
      const float m = gmax(fmaxf(fmaxf(fmaxf(mx[0], mx[1]), mx[2]), mx[3]));
      f32x4 o0 = {0.f, 0.f, 0.f, 0.f}, o1 = {0.f, 0.f, 0.f, 0.f};
#pragma unroll
      for(int i = 0; i < 17; ++i) {
        if(i + 4 < 17) vf[i + 4] = *(LAS v4u *)(kv + (i + 4) * 2048 + 1024);
        const f32x4 d = s[i] - m;
        v2u p;
        p.x = pk2(__builtin_amdgcn_exp2f(d[0]), __builtin_amdgcn_exp2f(d[1]));
        p.y = pk2(__builtin_amdgcn_exp2f(d[2]), __builtin_amdgcn_exp2f(d[3]));
        o0 = mfma16(v2u{vf[i].x, vf[i].y}, p, o0);
        o1 = mfma16(v2u{vf[i].z, vf[i].w}, p, o1);
        if(i & 1) SB();
      }
      const float inv = __builtin_amdgcn_rcpf(__shfl(o1[0], 48 + li));
      o0 *= inv, o1 *= inv;
      const v4u of = {pk2(o0[0], o0[1]), pk2(o0[2], o0[3]), pk2(o1[0], o1[1]), pk2(o1[2], o1[3])};
      st16(of, rws, lane * 16, OFF_O + layer * O_L + (tt * 16 + slot) * 1024);
      STAMP(9 + layer * 30);
      DRAIN();
      BARRIER();
      STAMP(10 + layer * 30);
      if(w == 7 && lane == 0) set_flag(ws, fl + FL_O + (t * 16 + slot) * 8, tag);
      WARM(1, OFF_O + layer * O_L + (tt * 16 + slot) * 1024, 16384);
      STAMP(11 + layer * 30);
    }
    {
      IDS();
      if(w == 7) {
        if(slot < 14) wait_flags(ws, fl + FL_O + (t * 16 + li) * 8, tag);
      } else {
        if(layer == 0 && slot < 14) WT_DMA(ID_WO + slot * 32, 0, 32, w, 7, L_WO);
        WT_DMA(ID_W1 + slot * 98, PF_T, PF_O, w, 7, L_X);
      }
      if(slot < 14) {
        const rsrc_t rx = layer == 0 ? __builtin_amdgcn_make_buffer_rsrc((void *)x, 0, S * D * 2, 0x00020000) : rws;
        const v4u xr = ld16(rx, layer == 0 ? (16 * tt + li) * (D * 2) + g * 16 : lane * 16, layer == 0 ? slot * 64 : OFF_X2 + (tt * 14 + slot) * 1024);
        BARRIER();
        STAMP(12 + layer * 30);
        v4u of[16];
        const int ob = opq(OFF_O + layer * O_L + tt * 16384);
#pragma unroll
        for(int h = 0; h < 8; ++h) of[h] = ld16(rws, lane * 16 + (h & 3) * 1024, ob + (h >> 2) * 4096);
        SB();
        if(layer == 0) {
          asm volatile("s_waitcnt vmcnt(8)" ::: "memory");
          BARRIER();
        }
        v4u wf[32];
#pragma unroll
        for(int i = 0; i < 8; ++i) wf[i] = *(LAS v4u *)(L + L_WO + i * 1024 + lane * 16);
        SB();
        f32x4 a0 = {0.f, 0.f, 0.f, 0.f}, a1 = {0.f, 0.f, 0.f, 0.f};
#pragma unroll
        for(int h = 0; h < 16; ++h) {
          if(h < 8) of[h + 8] = ld16(rws, lane * 16 + (h & 3) * 1024, ob + ((h + 8) >> 2) * 4096);
          if(h < 12) wf[2 * h + 8] = *(LAS v4u *)(L + L_WO + (2 * h + 8) * 1024 + lane * 16), wf[2 * h + 9] = *(LAS v4u *)(L + L_WO + (2 * h + 9) * 1024 + lane * 16);
          a0 = mfma32(wf[2 * h], of[h], a0), a1 = mfma32(wf[2 * h + 1], of[h], a1);
          SB();
        }
        const v4u y = {pk2(a0[0] + bflo(xr.x), a0[1] + bfhi(xr.x)), pk2(a0[2] + bflo(xr.y), a0[3] + bfhi(xr.y)),
            pk2(a1[0] + bflo(xr.z), a1[1] + bfhi(xr.z)), pk2(a1[2] + bflo(xr.w), a1[3] + bfhi(xr.w))};
        st16(y, rws, lane * 16, OFF_X1 + layer * X_L + (tt * 14 + slot) * 1024);
        st8(piece_stats(y), rws, lane < 16 ? lane * 8 : OOB, OFF_ST + layer * ST_L + tt * 2048 + slot * 128);
        STAMP(13 + layer * 30);
        DRAIN();
        BARRIER();
        if(w == 7 && lane == 0) set_flag(ws, fl + FL_X1 + (t * 16 + slot) * 8, tag);
        WARM(1, OFF_X1 + layer * X_L + (tt * 14 + slot) * 1024, 14336);
      }
      STAMP(14 + layer * 30);
    }
    {
      IDS();
      const int xb = opq(OFF_X1 + layer * X_L + tt * 14336);
      if(w == 7) wait_flags(ws, fl + FL_X1 + (t * 16 + (lane < 14 ? lane : 0)) * 8, tag);
      else WT_DMA(ID_W1 + slot * 98, PF_O, 98, w, 7, L_X);
      if(layer == 0) {
        W2_LOAD(0, W2_A);
        if(ti < 112) {
          const int nt = ti >> 4, i = ti & 15;
          float sum = 0.f;
#pragma unroll
          for(int ks = 0; ks < 14; ++ks)
            sum += __uint_as_float(__builtin_amdgcn_raw_buffer_load_b32(rws, OFF_CSP + (ID_W1 + slot * 98 + ks * 7) * 64 + ti * 4, 0, AUX_SC1));
          *(LAS float *)(L + LC_CS1 + ti * 4) = sum;
          const int col = nt < 6 ? 112 * slot + 32 * (nt >> 1) + 8 * (i >> 2) + (i & 3) + 4 * (nt & 1) : 112 * slot + 96 + i;
          *(LAS float *)(L + LC_B1 + ti * 4) = bflo(b1[col]);
        }
      }
      BARRIER();
      STAMP(15 + layer * 30);
      v4u xf[14];
      v2u sp[4];
      ROW_STATS(OFF_ST + layer * ST_L);
#pragma unroll
      for(int ks = 0; ks < XRING; ++ks) xf[ks] = ld16(rws, lane * 16 + (ks & 3) * 1024, xb + (ks >> 2) * 4096);
      SB();
      asm volatile("s_waitcnt vmcnt(%0)" ::"i"(4 + XRING) : "memory");
      BARRIER();
      f32x4 acc[7];
#pragma unroll
      for(int n = 0; n < 7; ++n) acc[n] = f32x4{0.f, 0.f, 0.f, 0.f};
      v4u wc[7], wn[7];
#pragma unroll
      for(int n = 0; n < 7; ++n) wn[n] = *(LAS v4u *)(L + L_X + n * 1024 + lane * 16);
      SUM_STATS();
      const float mu = s1 * (1.f / D), var = fmaxf(s2 * (1.f / D) - mu * mu, 0.f), r1 = __builtin_amdgcn_rsqf(var + EPS);
      const float rs = r1 * __builtin_amdgcn_rsqf(var * r1 * r1 + EPS), nmr = -mu * rs;
      const int wb = opq(OFF_WT + (ID_W2 + CSL(slot) * 224 + w * 28) * 1024);
      SB();
#define W1_GEMM(W2L) \
  _Pragma("unroll") for(int ks = 0; ks < 14; ++ks) { \
    _Pragma("unroll") for(int n = 0; n < 7; ++n) wc[n] = wn[n]; \
    if(ks + XRING < 14) xf[ks + XRING] = ld16(rws, lane * 16 + ((ks + XRING) & 3) * 1024, xb + ((ks + XRING) >> 2) * 4096); \
    else if(W2L) { \
      _Pragma("unroll") for(int i = W2_A + (ks + XRING - 14) * W2_S; i < W2_A + (ks + XRING - 13) * W2_S && i < W2_B; ++i) \
          w2f[i] = ld16(rws, lane * 16 + (i & 3) * 1024, wb + (i >> 2) * 4096); \
    } \
    _Pragma("unroll") for(int n = 0; n < 7; ++n) { \
      if(ks < 13) wn[n] = *(LAS v4u *)(L + L_X + ((ks + 1) * 7 + n) * 1024 + lane * 16); \
      acc[n] = mfma32(wc[n], xf[ks], acc[n]); \
      __builtin_amdgcn_sched_group_barrier(0x100, 1, 0); \
      __builtin_amdgcn_sched_group_barrier(0x8, 1, 0); \
    } \
    SB(); \
  }
      if(layer == 0) {
        W1_GEMM(true);
      } else {
        W1_GEMM(false);
      }
      STAMP(16 + layer * 30);
      const int ho = OFF_H + layer * H_L + tt * 57344;
      f32x4 cc[7], bb[7];
#pragma unroll
      for(int n = 0; n < 7; ++n) cc[n] = *(LAS f32x4 *)(L + LC_CS1 + n * 64 + g * 16), bb[n] = *(LAS f32x4 *)(L + LC_B1 + n * 64 + g * 16);
      SB();
      unsigned hp[14];
#pragma unroll
      for(int n = 0; n < 7; ++n) {
        const f32x4 v = acc[n] * rs + (cc[n] * nmr + bb[n]);
        const f32x4 a = v * (v * v * -0.1029432396f + -2.3022081986f);
        const f32x4 e = {__builtin_amdgcn_exp2f(a[0]), __builtin_amdgcn_exp2f(a[1]), __builtin_amdgcn_exp2f(a[2]), __builtin_amdgcn_exp2f(a[3])};
        const f32x4 d = e + 1.f;
        const f32x4 r = {__builtin_amdgcn_rcpf(d[0]), __builtin_amdgcn_rcpf(d[1]), __builtin_amdgcn_rcpf(d[2]), __builtin_amdgcn_rcpf(d[3])};
        const f32x4 hv = v * r;
        hp[2 * n] = pk2(hv[0], hv[1]), hp[2 * n + 1] = pk2(hv[2], hv[3]);
        if(n & 1) st16(v4u{hp[2 * n - 2], hp[2 * n - 1], hp[2 * n], hp[2 * n + 1]}, rws, lane * 16 + (n >> 1) * 1024, ho + slot * 3072);
      }
      st8(v2u{hp[12], hp[13]}, rws, lane * 16, ho + (48 + (slot >> 1)) * 1024 + (slot & 1) * 8);
      STAMP(17 + layer * 30);
      DRAIN();
      BARRIER();
      STAMP(18 + layer * 30);
      WARM(3, ho + slot * 3072, 57344);
      if(w == 7) {
        if(lane == 0) set_flag(ws, fl + FL_H + (t * 16 + slot) * 8, tag);
        if(slot < 14) wait_flags(ws, fl + FL_H + (t * 16 + li) * 8, tag);
      }
      if(layer == 0) {
        W2_LOAD(W2_B, 28);
        if(w < 7) WT_DMA(ID_WQ + slot * 84, 0, PF_WQ, w, 7, L_X);
      }
    }
    if(SLOT_OF(b0) < 14) {
      IDS();
      const int csl = CSL(slot), chalf = slot >= 7;
      const int tt0 = 8 * t + 4 * chalf;
      BARRIER();
      STAMP(19 + layer * 30);
      const int ho = opq(OFF_H + layer * H_L + tt0 * 57344 + w * 7168);
      v4u hf[8];
#pragma unroll
      for(int i = 0; i < 8; ++i) hf[i] = ld16(rws, lane * 16 + (i >> 2) * 1024, ho + (i & 3) * 57344);
      const int ott = tt0 + (w >> 1), oks = 2 * csl + (w & 1);
      v4u xr, bb;
      SB();
      f32x4 acc[4][4];
#pragma unroll
      for(int q = 0; q < 4; ++q)
#pragma unroll
        for(int n = 0; n < 4; ++n) acc[q][n] = f32x4{0.f, 0.f, 0.f, 0.f};
#pragma unroll
      for(int k = 0; k < 7; ++k) {
#pragma unroll
        for(int q = 0; q < 4; ++q) {
          const v4u cur = hf[(k & 1) * 4 + q];
          if(k + 2 < 7) hf[(k & 1) * 4 + q] = ld16(rws, lane * 16 + (k & 3) * 1024, ho + q * 57344 + 2048 + (k >> 2) * 4096);
          if(k == 5 && q == 1) xr = ld16(rws, lane * 16, opq(OFF_X1 + layer * X_L + (ott * 14 + oks) * 1024));
          if(k == 5 && q == 2)
            bb = __builtin_amdgcn_raw_buffer_load_b128(__builtin_amdgcn_make_buffer_rsrc((void *)b2, 0, D * 2, 0x00020000), g * 16, opq(oks * 64), 0);
#pragma unroll
          for(int n = 0; n < 4; ++n) acc[q][n] = mfma32(w2f[k * 4 + n], cur, acc[q][n]);
          SB();
        }
      }
      STAMP(20 + layer * 30);
      f32x4 a0, a1;
#pragma unroll
      for(int f = 0; f < 8; ++f) {
        const f32x4 p0 = acc[f >> 1][2 * (f & 1)], p1 = acc[f >> 1][2 * (f & 1) + 1];
        if(f == w) a0 = p0, a1 = p1;
        else
          *(LAS v4u *)(L + L_RED + (f * 7 + (w < f ? w : w - 1)) * 1024 + lane * 16) =
              v4u{pk2(p0[0], p0[1]), pk2(p0[2], p0[3]), pk2(p1[0], p1[1]), pk2(p1[2], p1[3])};
      }
      BARRIER();
      STAMP(21 + layer * 30);
      a0 += f32x4{bflo(xr.x) + bflo(bb.x), bfhi(xr.x) + bfhi(bb.x), bflo(xr.y) + bflo(bb.y), bfhi(xr.y) + bfhi(bb.y)};
      a1 += f32x4{bflo(xr.z) + bflo(bb.z), bfhi(xr.z) + bfhi(bb.z), bflo(xr.w) + bflo(bb.w), bfhi(xr.w) + bfhi(bb.w)};
#pragma unroll
      for(int s = 0; s < 7; ++s) {
        const v4u p = *(LAS v4u *)(L + L_RED + (w * 7 + s) * 1024 + lane * 16);
        a0 += f32x4{bflo(p.x), bfhi(p.x), bflo(p.y), bfhi(p.y)};
        a1 += f32x4{bflo(p.z), bfhi(p.z), bflo(p.w), bfhi(p.w)};
      }
      const v4u y = {pk2(a0[0], a0[1]), pk2(a0[2], a0[3]), pk2(a1[0], a1[1]), pk2(a1[2], a1[3])};
      if(layer == 0) {
        st16(y, rws, lane * 16, OFF_X2 + (ott * 14 + oks) * 1024);
        st8(piece_stats(y), rws, lane < 16 ? lane * 8 : OOB, OFF_ST + 2 * ST_L + ott * 2048 + oks * 128);
        STAMP(22 + layer * 30);
        DRAIN();
      } else {
        *(GAS v4u *)(out + (16 * ott + li) * D + 32 * oks + 8 * g) = y;
      }
    }
    STAMP(23 + layer * 30);
    if(layer == 0) {
      IDS();
      DRAIN();
      BARRIER();
      if(w == 7) {
        if(lane == 0 && slot < 14) set_flag(ws, OFF_FLAGS + FL_X2 + (t * 16 + slot) * 8, tag);
        wait_flags(ws, OFF_FLAGS + FL_X2 + (t * 16 + (lane < 14 ? lane : 0)) * 8, tag);
      } else {
        if(slot < 14) WARM(1, OFF_X2 + ((8 * t + 4 * (slot >= 7) + (w >> 1)) * 14 + 2 * CSL(slot) + (w & 1)) * 1024, 1024);
        WT_DMA(ID_WQ + slot * 84, PF_WQ, 84, w, 7, L_X);
      }
      BARRIER();
    }
  };
  run_layer(IntC<0>{});
  run_layer(IntC<1>{});
  STAMP(61);
  asm volatile("s_waitcnt vmcnt(0)" ::"v"(pv0), "v"(pv1) : "memory");
  asm volatile(".Lfk_end:");
}
}

extern "C" void kernel_launch(void *const *d_in, const int *in_sizes, int n_in, void *d_out, int out_size, void *d_ws, size_t ws_size,
    hipStream_t stream) {
  const size_t WS_BASE = 0;
  fused_kernel<<<NWG, NTHR, 0, stream>>>((const u16 *)d_in[0], (const u16 *)d_in[1], (const u16 *)d_in[2], (const u16 *)d_in[4],
      (const u16 *)d_in[5], (const u16 *)d_in[6], (const u16 *)d_in[7], (u16 *)d_out, (unsigned char *)d_ws + WS_BASE);
}
```

```cpp
#include <hip/hip_runtime.h>
#include <stdint.h>

#define GAS __attribute__((address_space(1)))
#define LAS __attribute__((address_space(3)))
#define DI __device__ __forceinline__
typedef unsigned v4u __attribute__((ext_vector_type(4)));
typedef unsigned v2u __attribute__((ext_vector_type(2)));
typedef float f32x4 __attribute__((ext_vector_type(4)));
typedef float f32x2 __attribute__((ext_vector_type(2)));
typedef __bf16 bf16x8 __attribute__((ext_vector_type(8)));
typedef __bf16 bf16x2 __attribute__((ext_vector_type(2)));
typedef short s16x4 __attribute__((ext_vector_type(4)));
typedef unsigned long long u64;
typedef unsigned short u16;
typedef __amdgpu_buffer_rsrc_t rsrc_t;

#define TILE_OF(b) ((((b)&7) >> 1) * 4 + ((b) >> 6))
#define SLOT_OF(b) (((b)&1) * 8 + (((b) >> 3) & 7))
#define CS14(s) ((s) >= 14 ? 0 : (s))
#define CSL(s) ((s) >= 14 ? 0 : (s) >= 7 ? (s)-7 : (s))

namespace {
template <int V> struct IntC {
  static constexpr int value = V;
};
constexpr int S = 2048, D = 448, F = 1792;
constexpr int NWG = 256, NTHR = 512;
constexpr float EPS = 1e-5f;
constexpr int AUX_SC1 = 16;
constexpr unsigned SPIN_CAP = 1u << 16;
constexpr int OOB = 0x7F000000;
constexpr int XRING = 8;
constexpr int WDEP = 7;
constexpr u64 VOTE = 1ull << 63;

constexpr int FS = 128;
constexpr int OFF_EPOCH = 0, OFF_PREPG = 4096, OFF_PREPF = 8192, OFF_SEL = 49152;
constexpr int OFF_CSP = 1 << 20;
constexpr int OFF_WT = 2 << 20;
constexpr int ID_WO = 0, ID_W1 = 448, ID_W2 = 2016, ID_WQ = 3584, ID_END = 4928;
constexpr int OFF_PROBE = 12 << 20, OFF_STAMP = 14 << 20;
constexpr int PG0 = 8, WS_BYTES = (PG0 + 48) << 21;
constexpr int F_KV = 0, F_O = 256, F_X1 = 384, F_H = 512, F_X2 = 0, F_ST = 128, F_FL = 192;
constexpr int FL_KV = 0, FL_O = 1, FL_X1 = 2, FL_H = 3, FL_X2 = 4;
#define FO(f) ((((f) >> 3) << 14) + (((f)&7) << 10))
#define PB(t_, p_) (((PG0 + 3 * (t_) + (p_)) << 21) + (((side ^ (t_) ^ (p_) ^ ((t_) >> 3)) & 1) << 13))
#define FLAG(t_, layer_, ph_, i_) (PB(t_, 2) + FO(F_FL + ((layer_)*5 + (ph_)) * 2) + (i_)*FS)
#define WT(f) (OFF_WT + (f)*1024)
static_assert(OFF_CSP + ID_END * 64 <= OFF_WT && OFF_WT + ID_END * 1024 <= OFF_PROBE && (PG0 & 1) == 0, "map");

constexpr int L_X = 0;
constexpr int L_KV = 100352 - 32768;
constexpr int L_WO = 100352 + 16384;
constexpr int L_RED = 86016;
constexpr int L_C = L_WO + 32768;
constexpr int LC_CSQ = L_C, LC_CS1 = L_C + 512, LC_B1 = L_C + 1024;
constexpr int LS_Q = L_C + 1536, LS_HOLD = LS_Q + 4, LS_KV = LS_Q + 8, LS_SIDE = LS_Q + 32;
constexpr int LDS_BYTES = L_C + 2048;
constexpr int W1_EARLY = 63;
constexpr int WQ_HOP = 28;
static_assert(W1_EARLY * 1024 <= L_KV && L_RED + 56 * 1024 <= L_C, "LDS map");

DI f32x4 mfma32(v4u a, v4u b, f32x4 c) {
  return __builtin_amdgcn_mfma_f32_16x16x32_bf16(__builtin_bit_cast(bf16x8, a), __builtin_bit_cast(bf16x8, b), c, 0, 0, 0);
}
DI f32x4 mfma16(v2u a, v2u b, f32x4 c) {
  return __builtin_amdgcn_mfma_f32_16x16x16bf16_1k(__builtin_bit_cast(s16x4, a), __builtin_bit_cast(s16x4, b), c, 0, 0, 0);
}
DI unsigned pk2(float a, float b) {
  f32x2 v = {a, b};
  return __builtin_bit_cast(unsigned, __builtin_convertvector(v, bf16x2));
}
DI float bflo(unsigned u) { return __uint_as_float(u << 16); }
DI float bfhi(unsigned u) { return __uint_as_float(u & 0xFFFF0000u); }
DI float dot2(unsigned a, unsigned b, float c) {
  return __builtin_amdgcn_fdot2_f32_bf16(__builtin_bit_cast(bf16x2, a), __builtin_bit_cast(bf16x2, b), c, false);
}
DI float gsum(float x) {
  auto r = __builtin_amdgcn_permlane32_swap(__float_as_uint(x), __float_as_uint(x), false, false);
  x = __uint_as_float(r[0]) + __uint_as_float(r[1]);
  auto q = __builtin_amdgcn_permlane16_swap(__float_as_uint(x), __float_as_uint(x), false, false);
  return __uint_as_float(q[0]) + __uint_as_float(q[1]);
}
DI float gmax(float x) {
  auto r = __builtin_amdgcn_permlane32_swap(__float_as_uint(x), __float_as_uint(x), false, false);
  x = fmaxf(fmaxf(x, __uint_as_float(r[0])), __uint_as_float(r[1]));
  auto q = __builtin_amdgcn_permlane16_swap(__float_as_uint(x), __float_as_uint(x), false, false);
  return fmaxf(fmaxf(x, __uint_as_float(q[0])), __uint_as_float(q[1]));
}
DI v4u ld16(rsrc_t rs, int voff, int soff) { return __builtin_amdgcn_raw_buffer_load_b128(rs, voff, soff, AUX_SC1); }
DI v2u ld8(rsrc_t rs, int voff, int soff) { return __builtin_amdgcn_raw_buffer_load_b64(rs, voff, soff, AUX_SC1); }
DI void st16(v4u v, rsrc_t rs, int voff, int soff) {
  __builtin_amdgcn_raw_buffer_store_b128(v, rs, voff, soff, AUX_SC1);
  asm volatile("s_nop 1" ::"v"(v) : "memory");
}
DI void st8(v2u v, rsrc_t rs, int voff, int soff) { __builtin_amdgcn_raw_buffer_store_b64(v, rs, voff, soff, AUX_SC1); }
DI int opq(int v) {
  asm volatile("" : "+s"(v));
  return v;
}
DI int opqv(int v) {
  asm volatile("" : "+v"(v));
  return v;
}
DI v2u trread(LAS unsigned char *p) { return __builtin_bit_cast(v2u, __builtin_amdgcn_ds_read_tr16_b64_v4i16((LAS s16x4 *)p)); }
#define DRAIN() asm volatile("s_waitcnt vmcnt(0)" ::: "memory")
#define BARRIER() asm volatile("s_waitcnt lgkmcnt(0)\n\ts_barrier" ::: "memory")
#define SB() __builtin_amdgcn_sched_barrier(0)
#define dma1k(rs, voff, soff, ldsaddr) \
  asm volatile("s_mov_b32 m0, %0\n\ts_nop 0\n\tbuffer_load_dwordx4 %1, %2, %3 offen sc1 lds" ::"s"((unsigned)(ldsaddr)), "v"((int)(voff)), \
               "s"(rs), "s"((int)(soff)) \
               : "memory")
#define dma1k_input(rs, voff, soff, ldsaddr) \
  asm volatile("s_mov_b32 m0, %0\n\ts_nop 0\n\tbuffer_load_dwordx4 %1, %2, %3 offen lds" ::"s"((unsigned)(ldsaddr)), "v"((int)(voff)), \
               "s"(rs), "s"((int)(soff)) \
               : "memory")
DI void wait_flags(const unsigned char *ws, int off, u64 tag) {
  const GAS u64 *p = (const GAS u64 *)(ws + off);
  for(unsigned spin = 0; spin < SPIN_CAP; ++spin) {
    const u64 v = __hip_atomic_load(p, __ATOMIC_RELAXED, __HIP_MEMORY_SCOPE_AGENT);
    if(__all(v == tag)) break;
    __builtin_amdgcn_s_sleep(1);
  }
  __builtin_amdgcn_fence(__ATOMIC_ACQUIRE, "wavefront");
}
DI void set_word(unsigned char *ws, int off, u64 v) {
  __hip_atomic_store((GAS u64 *)(ws + off), v, __ATOMIC_RELAXED, __HIP_MEMORY_SCOPE_AGENT);
}
DI unsigned lds_count(LAS unsigned char *p, unsigned add, int lane) {
  unsigned old = 0;
  if(lane == 0) old = __hip_atomic_fetch_add((LAS unsigned *)p, add, __ATOMIC_RELAXED, __HIP_MEMORY_SCOPE_WORKGROUP);
  return __builtin_amdgcn_readfirstlane(old);
}
DI v2u piece_stats(v4u y) {
  float s1 = 0.f, s2 = 0.f;
#pragma unroll
  for(int j = 0; j < 4; ++j) s1 = dot2(y[j], 0x3F803F80u, s1), s2 = dot2(y[j], y[j], s2);
  return v2u{__float_as_uint(gsum(s1)), __float_as_uint(gsum(s2))};
}

#define STAMP(i)

__global__ void __launch_bounds__(NTHR, 2)
    fused_kernel(const u16 *__restrict__ x, const u16 *__restrict__ wqkv, const u16 *__restrict__ wo, const u16 *__restrict__ w1,
        const u16 *__restrict__ b1, const u16 *__restrict__ w2, const u16 *__restrict__ b2, u16 *__restrict__ out, unsigned char *ws) {
  __shared__ __attribute__((aligned(16))) unsigned char smem[LDS_BYTES];
  LAS unsigned char *L = (LAS unsigned char *)smem;
  const unsigned ldsb = (unsigned)(size_t)L;
  const int tid = threadIdx.x, w0 = __builtin_amdgcn_readfirstlane(tid >> 6), b0 = blockIdx.x;
#define IDS() \
  const int ti = opqv(tid), lane = ti & 63, w = opq(w0), b = opq(b0), t = TILE_OF(b), slot = SLOT_OF(b); \
  const int tt = 8 * t + w, g = lane >> 4, li = lane & 15; \
  (void)g, (void)li, (void)tt, (void)slot
#define WT_DMA(id0, from, to, w_, nw_, ldsoff) \
  for(int i_ = (from) + (w_); i_ < (to); i_ += (nw_)) dma1k(rws, lane * 16, WT((id0) + i_), ldsb + (ldsoff) + i_ * 1024)
#define GEMM_PART(NT, N0, NN, K0, K1, SWAPN, STEP) \
  { \
    constexpr int CNT = ((K1) - (K0)) * (NN); \
    v4u wr[WDEP]; \
    _Pragma("unroll") for(int i = 0; i < WDEP; ++i) wr[i] = *(LAS v4u *)(L + L_X + (((K0) + i / (NN)) * (NT) + (N0) + i % (NN)) * 1024 + lane * 16); \
    SB(); \
    _Pragma("unroll") for(int i = 0; i < CNT; ++i) { \
      const int ks = (K0) + i / (NN), n = (N0) + i % (NN); \
      if(i % (NN) == 0) { \
        STEP(ks); \
      } \
      const v4u cur = wr[i % WDEP]; \
      if(i + WDEP < CNT) \
        wr[i % WDEP] = *(LAS v4u *)(L + L_X + (((K0) + (i + WDEP) / (NN)) * (NT) + (N0) + (i + WDEP) % (NN)) * 1024 + lane * 16); \
      acc[n] = n < (SWAPN) ? mfma32(cur, xf[ks], acc[n]) : mfma32(xf[ks], cur, acc[n]); \
      SB(); \
    } \
  }
#define X4(k) ((((k) >> 3) << 14) + ((((k) >> 2) & 1) << 12))
#define XLOAD(k) xf[k] = ld16(rws, lane * 16 + ((k)&3) * 1024, xb + X4(k))
#define STEP_NONE(ks)
#define STEP_X(ks) \
  if((ks) + XRING < 14) XLOAD((ks) + XRING)
#define ROW_STATS(which) \
  _Pragma("unroll") for(int i = 0; i < 4; ++i) \
      sp[i] = ld8(rws, (i == 3 && g > 1) ? OOB : li * 8 + g * 128 + i * 512, PB(t, 2) + FO(F_ST + ((which)*8 + w) * 2))
#define SUM_STATS() \
  const float s1 = gsum((__uint_as_float(sp[0].x) + __uint_as_float(sp[1].x)) + (__uint_as_float(sp[2].x) + __uint_as_float(sp[3].x))); \
  const float s2 = gsum((__uint_as_float(sp[0].y) + __uint_as_float(sp[1].y)) + (__uint_as_float(sp[2].y) + __uint_as_float(sp[3].y)))
#define W2LOAD(j) w2f[j] = ld16(rws, lane * 16 + ((j)&3) * 1024, WT(wb + ((j) & ~3)))
  const rsrc_t rws = __builtin_amdgcn_make_buffer_rsrc((void *)ws, 0, WS_BYTES, 0x00020000);
  STAMP(0);
  u64 tag = 0;
  int side = 0;
  v4u w2f[28];

  auto run_layer = [&](auto LC) __attribute__((always_inline)) {
    constexpr int layer = decltype(LC)::value;
    v4u qf;
    {
      IDS();
      v4u xf[14];
      float mu, rs, mur[4], rsr[4];
      f32x4 acc[6];
#pragma unroll
      for(int n = 0; n < 6; ++n) acc[n] = f32x4{0.f, 0.f, 0.f, 0.f};
#define A_FINISH_STATS() \
  mu = s1 * (1.f / D), rs = __builtin_amdgcn_rsqf(fmaxf(s2 * (1.f / D) - mu * mu, 0.f) + EPS); \
  _Pragma("unroll") for(int r = 0; r < 4; ++r) mur[r] = __shfl(mu, 4 * g + r), rsr[r] = __shfl(rs, 4 * g + r); \
  asm volatile("" : "+v"(mu), "+v"(rs), "+v"(mur[0]), "+v"(mur[1]), "+v"(mur[2]), "+v"(mur[3]), "+v"(rsr[0]), "+v"(rsr[1]), "+v"(rsr[2]), "+v"(rsr[3]))
      if(layer == 0) {
        v2u ep;
        asm volatile("buffer_load_dwordx2 %0, off, %1, 0 sc1" : "=v"(ep) : "s"(rws) : "memory");
        int dst[2], dst1[2], tro[2], tre[2];
        if(ti < 8) *(LAS unsigned *)(L + LS_Q + 4 * ti) = 0u;
        const rsrc_t rx = __builtin_amdgcn_make_buffer_rsrc((void *)x, 0, S * D * 2, 0x00020000);
        const int xvo = (16 * tt + (lane >> 3)) * (D * 2) + (((lane & 7) ^ (lane >> 3)) * 16);
#define X0_DMA() \
  _Pragma("unroll") for(int i = 0; i < 14; ++i) dma1k_input(rx, xvo, (i & 1) * 8 * (D * 2) + (i >> 1) * 128, ldsb + w * 14336 + i * 1024)
#define TAKE_TAG() \
  asm volatile("s_waitcnt vmcnt(0)" : "+v"(ep)::"memory"); \
  tag = (((u64)(unsigned)__builtin_amdgcn_readfirstlane(ep.y) << 32) | (unsigned)__builtin_amdgcn_readfirstlane(ep.x)) + 1ull
#define PREP_OUT(rd, QPAD) \
  { \
    LAS unsigned char *p = L + L_WO + ((rd)*8 + w) * 2048 + (8 * g + (li >> 2)) * 64 + (li & 3) * tro[rd]; \
    const v2u a0 = trread(p), a1 = trread(p + 256), c0 = trread(p + tre[rd]), c1 = trread(p + tre[rd] + 256); \
    const v4u f0 = {a0.x, a0.y, a1.x, a1.y}; \
    v4u f1 = {c0.x, c0.y, c1.x, c1.y}; \
    if(QPAD && li >= 12) f1 = v4u{0u, 0u, 0u, 0u}; \
    const bool ok = dst[rd] < ID_END; \
    st16(f0, rws, ok ? lane * 16 : OOB, WT(dst[rd])); \
    st16(f1, rws, ok ? lane * 16 : OOB, WT(dst1[rd])); \
    float ca = 0.f, cb = 0.f; \
    _Pragma("unroll") for(int j = 0; j < 4; ++j) ca = dot2(f0[j], 0x3F803F80u, ca), cb = dot2(f1[j], 0x3F803F80u, cb); \
    ca = gsum(ca), cb = gsum(cb); \
    __builtin_amdgcn_raw_buffer_store_b32( \
        __float_as_uint(g ? cb : ca), rws, (ok && g < 2) ? OFF_CSP + (g ? dst1[rd] : dst[rd]) * 64 + li * 4 : OOB, 0, AUX_SC1); \
  }
        BARRIER();
        if(w < 3) {
          unsigned vote = 1u;
          if(b >= 224) {
            unsigned v = 0;
            unsigned char *pp = ws + OFF_PROBE + (b - 224) * 16384 + w * 512;
            asm volatile("global_store_dword %0, %1, off sc1\n\ts_waitcnt vmcnt(0)" ::"v"(pp + (lane & 1) * 8192 + ((lane >> 1) & 1) * 128), "v"(v)
                         : "memory");
            unsigned best[2] = {~0u, ~0u};
#pragma unroll
            for(int i = 0; i < 4; ++i) {
              u64 t0, t1;
              asm volatile(
                  "s_memtime %0\n\ts_waitcnt lgkmcnt(0)\n\tglobal_load_dword %2, %3, off sc1\n\ts_waitcnt vmcnt(0)\n\ts_memtime %1\n\ts_waitcnt lgkmcnt(0)"
                  : "=&s"(t0), "=&s"(t1), "=&v"(v)
                  : "v"(pp + ((i ^ w ^ (b >> 3)) & 1) * 8192 + (i >> 1) * 128)
                  : "memory");
              const unsigned dt = (unsigned)(t1 - t0);
              if((i ^ w ^ (b >> 3)) & 1) best[1] = min(best[1], dt);
              else best[0] = min(best[0], dt);
            }
            if((best[1] < best[0]) != (bool)((b >> 2) & 1)) vote = 0x101u;
          }
          const int r = lane >> 2, c = lane & 3, head = b / 14, ks = b - 14 * head;
          const rsrc_t rq = __builtin_amdgcn_make_buffer_rsrc((void *)wqkv, 0, D * 3 * D * 2, 0x00020000);
          const int o0 = (32 * ks + r) * 2688 + w * 896 + 56 * head + c * 16;
          dma1k_input(rq, o0, 0, ldsb + L_WO + w * 2048);
          dma1k_input(rq, o0, 16 * 2688, ldsb + L_WO + w * 2048 + 1024);
          dst[0] = b < 224 ? ID_WQ + head * 84 + ks * 6 + w * 2 : ID_END, dst1[0] = dst[0] + 1, tro[0] = 16, tre[0] = 8;
          TAKE_TAG();
          STAMP(25);
          PREP_OUT(0, true);
          lds_count(L + LS_HOLD, 1u, lane);
          DRAIN();
          STAMP(26);
          const unsigned cnt = lds_count(L + LS_Q, vote, lane) + vote;
          if((cnt & 0xFFu) == 3u && lane == 0) set_word(ws, OFF_PREPF + b * FS, cnt >= 0x200u ? tag ^ VOTE : tag);
          X0_DMA();
          DRAIN();
        } else {
#pragma unroll
          for(int rd = 0; rd < 2; ++rd) {
            const int id = rd == 0 ? b * 5 + (w - 3) : w < 5 ? 1280 + b * 2 + (w - 3) : 1792, r = lane >> 2, c = lane & 3;
            const u16 *src;
            int bytes, o0, o1;
            tro[rd] = 16, tre[rd] = 8;
            if(id < 224) {
              const int ks = id / 14, sl = id - ks * 14;
              src = wo, bytes = D * D * 2;
              o0 = (28 * ks + r) * 896 + 64 * sl + c * 16, o1 = r < 12 ? o0 + 16 * 896 : OOB;
              dst[rd] = ID_WO + sl * 32 + ks * 2, dst1[rd] = dst[rd] + 1;
            } else if(id < 224 + 672) {
              const int i2 = id - 224, ks = i2 / 48, rem = i2 - ks * 48, sl = rem / 3, p = rem - sl * 3;
              src = w1, bytes = D * F * 2;
              o0 = (32 * ks + r) * 3584 + 224 * sl + 64 * p + c * 16, o1 = o0 + 16 * 3584;
              dst[rd] = ID_W1 + sl * 98 + ks * 7 + 2 * p, dst1[rd] = dst[rd] + 1;
            } else if(id < 896 + 112) {
              const int i2 = id - 896, ks = i2 >> 3, sl = 2 * (i2 & 7);
              src = w1, bytes = D * F * 2;
              o0 = (32 * ks + r) * 3584 + 224 * (sl + (c >> 1)) + 192 + (c & 1) * 16, o1 = o0 + 16 * 3584;
              dst[rd] = ID_W1 + sl * 98 + ks * 7 + 6, dst1[rd] = dst[rd] + 98, tro[rd] = 8, tre[rd] = 32;
            } else {
              const int i2 = id - 1008, hks = i2 / 14, cb = i2 - hks * 14, wv = hks / 7, kk = hks - 7 * wv;
              src = w2, bytes = F * D * 2;
              const int row = 224 * wv +
                  (kk < 4 ? 112 * (kk >> 1) + 32 * (kk & 1) + r : kk < 6 ? 112 * (kk - 4) + 64 + r : 96 + 112 * ((r >> 2) & 1) + 4 * (r >> 3) + (r & 3));
              o0 = row * 896 + 64 * cb + c * 16, o1 = o0 + (kk < 6 ? 16 : 8) * 896;
              dst[rd] = ID_W2 + (cb >> 1) * 224 + hks * 4 + (cb & 1) * 2, dst1[rd] = dst[rd] + 1;
              if(id >= 1792) o0 = o1 = OOB, dst[rd] = dst1[rd] = ID_END;
            }
            const rsrc_t rsw = __builtin_amdgcn_make_buffer_rsrc((void *)src, 0, bytes, 0x00020000);
            asm volatile("s_nop 4" ::: "memory");
            dma1k_input(rsw, o0, 0, ldsb + L_WO + (rd * 8 + w) * 2048);
            dma1k_input(rsw, o1, 0, ldsb + L_WO + (rd * 8 + w) * 2048 + 1024);
          }
          for(unsigned spin = 0; spin < (1u << 20); ++spin)
            if(__builtin_amdgcn_readfirstlane(__hip_atomic_load((LAS unsigned *)(L + LS_HOLD), __ATOMIC_RELAXED, __HIP_MEMORY_SCOPE_WORKGROUP)) >= 3)
              break;
          X0_DMA();
          STAMP(25);
          TAKE_TAG();
          STAMP(26);
          if(w == 7) wait_flags(ws, OFF_PREPF + (slot * 14 + (li < 14 ? li : 0)) * FS, tag);
        }
        STAMP(27);
#pragma unroll
        for(int ks = 0; ks < 14; ++ks)
          xf[ks] = *(LAS v4u *)(L + w * 14336 + ((ks >> 1) * 2 + (li >> 3)) * 1024 + (li & 7) * 128 + (((ks & 1) * 4 + g) ^ (li & 7)) * 16);
        BARRIER();
        STAMP(2);
        WT_DMA(ID_WQ + slot * 84, 0, 84, w, 8, L_X);
        {
          float s1 = 0.f, s2 = 0.f, s3 = 0.f, s4 = 0.f;
#pragma unroll
          for(int ks = 0; ks < 14; ++ks) {
            s1 = dot2(xf[ks][0], 0x3F803F80u, s1), s2 = dot2(xf[ks][0], xf[ks][0], s2);
            s3 = dot2(xf[ks][1], 0x3F803F80u, s3), s4 = dot2(xf[ks][1], xf[ks][1], s4);
            s1 = dot2(xf[ks][2], 0x3F803F80u, s1), s2 = dot2(xf[ks][2], xf[ks][2], s2);
            s3 = dot2(xf[ks][3], 0x3F803F80u, s3), s4 = dot2(xf[ks][3], xf[ks][3], s4);
          }
          s1 = gsum(s1 + s3), s2 = gsum(s2 + s4);
          A_FINISH_STATS();
        }
        asm volatile("s_waitcnt vmcnt(5)" ::: "memory");
        BARRIER();
        STAMP(3);
        if(w >= 3) {
          PREP_OUT(0, false);
          PREP_OUT(1, false);
        }
        if(ti < 96) {
          float csum = 0.f;
#pragma unroll
          for(int ks = 0; ks < 14; ++ks)
            csum += __uint_as_float(__builtin_amdgcn_raw_buffer_load_b32(rws, OFF_CSP + (ID_WQ + slot * 84 + ks * 6) * 64 + ti * 4, 0, AUX_SC1));
          *(LAS float *)(L + LC_CSQ + ti * 4) = csum;
        }
        GEMM_PART(6, 2, 4, 0, 7, 4, STEP_NONE);
        if(w == 1) {
          u64 v = 0;
          for(unsigned spin = 0; spin < SPIN_CAP; ++spin) {
            v = __hip_atomic_load((const GAS u64 *)(ws + OFF_PREPF + (224 + (lane & 31)) * FS), __ATOMIC_RELAXED, __HIP_MEMORY_SCOPE_AGENT);
            if(__all((v | VOTE) == (tag | VOTE))) break;
          }
          const int ones = __popcll(__ballot(v != tag));
          if(lane == 0) *(LAS int *)(L + LS_SIDE) = ones > 32;
        }
        DRAIN();
        BARRIER();
        side = __builtin_amdgcn_readfirstlane(*(LAS int *)(L + LS_SIDE));
        if(ti == 0) set_word(ws, OFF_PREPG + b * 8, tag);
        GEMM_PART(6, 2, 4, 7, 14, 4, STEP_NONE);
      } else {
        STAMP(31);
        const int xb = opq(PB(t, 2) + FO(F_X2 + w * 16));
        v2u sp[4];
        ROW_STATS(2);
#pragma unroll
        for(int ks = 0; ks < XRING; ++ks) XLOAD(ks);
        SB();
        STAMP(33);
#define STEP_XA(ks) \
  if((ks) == 3) { \
    SUM_STATS(); \
    A_FINISH_STATS(); \
  } \
  STEP_X(ks)
        GEMM_PART(6, 2, 4, 0, 14, 4, STEP_XA);
      }
      STAMP(4 + layer * 30);
      const float qs = rs * (0.18898223650461362f * 1.4426950408889634f);
      f32x4 c[4];
#pragma unroll
      for(int n = 0; n < 4; ++n) c[n] = *(LAS f32x4 *)(L + LC_CSQ + n * 64 + g * 16);
      const float c4 = *(LAS float *)(L + LC_CSQ + 256 + li * 4), c5 = *(LAS float *)(L + LC_CSQ + 320 + li * 4);
      f32x4 a0 = (acc[2] - mu * c[2]) * rs, a1 = (acc[3] - mu * c[3]) * rs;
      const v4u kf = {pk2(a0[0], a0[1]), pk2(a0[2], a0[3]), g == 3 ? 0u : pk2(a1[0], a1[1]), g == 3 ? 0u : pk2(a1[2], a1[3])};
#pragma unroll
      for(int r = 0; r < 4; ++r) a0[r] = (acc[4][r] - mur[r] * c4) * rsr[r], a1[r] = (acc[5][r] - mur[r] * c5) * rsr[r];
      const unsigned vpad = li == 12 ? 0x3F803F80u : 0u;
      const v4u vf = {pk2(a0[0], a0[1]), pk2(a0[2], a0[3]), li >= 12 ? vpad : pk2(a1[0], a1[1]), li >= 12 ? vpad : pk2(a1[2], a1[3])};
      const int kvo = PB(t, layer) + FO(F_KV + (slot * 8 + w) * 2);
      st16(kf, rws, lane * 16, kvo);
      st16(vf, rws, lane * 16 + 1024, kvo);
      *(LAS v4u *)(L + L_KV + (16 + w) * 2048 + lane * 16) = kf;
      *(LAS v4u *)(L + L_KV + (16 + w) * 2048 + 1024 + lane * 16) = vf;
      STAMP(5 + layer * 30);
#define STEP_KVF(ks) \
  if((ks) == 9) { \
    DRAIN(); \
    if(lds_count(L + LS_KV, 1u, lane) == 8u * (layer + 1) - 1u && lane == 0) set_word(ws, FLAG(t, layer, FL_KV, slot), tag); \
    SB(); \
  }
      GEMM_PART(6, 0, 2, 0, 14, 4, STEP_KVF);
      a0 = (acc[0] - mu * c[0]) * qs, a1 = (acc[1] - mu * c[1]) * qs;
      qf = v4u{pk2(a0[0], a0[1]), pk2(a0[2], a0[3]), g == 3 ? 0x00003F80u : pk2(a1[0], a1[1]), g == 3 ? 0u : pk2(a1[2], a1[3])};
    }
    BARRIER();
    {
      IDS();
      if(layer == 0 && w == 6) {
        for(unsigned spin = 0; spin < SPIN_CAP; ++spin) {
          const v4u p0 = ld16(rws, OFF_PREPG + lane * 32, 0), p1 = ld16(rws, OFF_PREPG + lane * 32 + 16, 0);
          const unsigned tl = (unsigned)tag, th = (unsigned)(tag >> 32);
          if(__all(p0.x == tl && p0.z == tl && p1.x == tl && p1.z == tl && p0.y == th && p0.w == th && p1.y == th && p1.w == th)) break;
          __builtin_amdgcn_s_sleep(1);
          asm volatile("" ::: "memory");
        }
        __builtin_amdgcn_fence(__ATOMIC_ACQUIRE, "wavefront");
        if(b == 0 && lane == 0) set_word(ws, OFF_EPOCH, tag);
      }
      STAMP(6 + layer * 30);
      if(w == 7 && t > 0) wait_flags(ws, ((lane & 1) && t > 1) ? FLAG(t - 2, layer, FL_KV, slot) : FLAG(t - 1, layer, FL_KV, slot), tag);
      BARRIER();
      STAMP(7 + layer * 30);
#pragma unroll
      for(int i = 0; i < 4; ++i) {
        const int pc = i * 8 + w;
        if(16 * t - 32 + pc >= 0) dma1k(rws, lane * 16, PB(t - 2 + (i >> 1), layer) + FO(F_KV + slot * 16 + (pc & 15)), ldsb + L_KV + pc * 1024);
        else *(LAS v4u *)(L + L_KV + pc * 1024 + lane * 16) = v4u{0u, 0u, (g == 3 && !(pc & 1)) ? 0x0000F149u : 0u, 0u};
      }
      f32x4 c0, c16;
#pragma unroll
      for(int r = 0; r < 4; ++r) c0[r] = (4 * g + r > li) ? 0.f : -1e30f, c16[r] = (4 * g + r <= li) ? 0.f : -1e30f;
      DRAIN();
      BARRIER();
      STAMP(8 + layer * 30);
      LAS unsigned char *kv = L + L_KV + w * 2048 + lane * 16;
      const int wb = opq(ID_W2 + CSL(slot) * 224 + w * 28);
      f32x4 s[17];
      {
        v4u kf[17];
#pragma unroll
        for(int i = 0; i < 6; ++i) kf[i] = *(LAS v4u *)(kv + i * 2048);
        SB();
#pragma unroll
        for(int i = 0; i < 17; ++i) {
          if(i + 6 < 17) kf[i + 6] = *(LAS v4u *)(kv + (i + 6) * 2048);
          s[i] = mfma32(kf[i], qf, i == 0 ? c0 : i == 16 ? c16 : f32x4{0.f, 0.f, 0.f, 0.f});
          if((i & 3) == 1 && i < 16)
            dma1k(rws, lane * 16, WT(ID_WO + CS14(slot) * 32 + (i >> 2) * 8 + w), ldsb + L_WO + ((i >> 2) * 8 + w) * 1024);
          SB();
        }
      }
      v4u vf[17];
#pragma unroll
      for(int i = 0; i < 4; ++i) vf[i] = *(LAS v4u *)(kv + i * 2048 + 1024);
      float mx[4] = {-1e30f, -1e30f, -1e30f, -1e30f};
#pragma unroll
      for(int i = 0; i < 17; ++i) {
        mx[(2 * i) & 3] = fmaxf(fmaxf(mx[(2 * i) & 3], s[i][0]), s[i][1]);
        mx[(2 * i + 1) & 3] = fmaxf(fmaxf(mx[(2 * i + 1) & 3], s[i][2]), s[i][3]);
      }
      const float m = gmax(fmaxf(fmaxf(fmaxf(mx[0], mx[1]), mx[2]), mx[3]));
      f32x4 o0 = {0.f, 0.f, 0.f, 0.f}, o1 = {0.f, 0.f, 0.f, 0.f};
#pragma unroll
      for(int i = 0; i < 17; ++i) {
        if(i + 4 < 17) vf[i + 4] = *(LAS v4u *)(kv + (i + 4) * 2048 + 1024);
        const f32x4 d = s[i] - m;
        v2u p;
        p.x = pk2(__builtin_amdgcn_exp2f(d[0]), __builtin_amdgcn_exp2f(d[1]));
        p.y = pk2(__builtin_amdgcn_exp2f(d[2]), __builtin_amdgcn_exp2f(d[3]));
        o0 = mfma16(v2u{vf[i].x, vf[i].y}, p, o0);
        o1 = mfma16(v2u{vf[i].z, vf[i].w}, p, o1);
        if(i < 8 && i * 8 + w < W1_EARLY) dma1k(rws, lane * 16, WT(ID_W1 + slot * 98 + i * 8 + w), ldsb + L_X + (i * 8 + w) * 1024);
        if(layer == 0 && i >= 8 && i < 16) W2LOAD(i - 8);
        if(i & 1) SB();
      }
      const float inv = __builtin_amdgcn_rcpf(__shfl(o1[0], 48 + li));
      o0 *= inv, o1 *= inv;
      const v4u of = {pk2(o0[0], o0[1]), pk2(o0[2], o0[3]), pk2(o1[0], o1[1]), pk2(o1[2], o1[3])};
      st16(of, rws, lane * 16, PB(t, layer) + FO(F_O + w * 16 + slot));
      STAMP(9 + layer * 30);
      DRAIN();
      BARRIER();
      STAMP(10 + layer * 30);
      if(w == 7 && lane == 0) set_word(ws, FLAG(t, layer, FL_O, slot), tag);
    }
    if(SLOT_OF(b0) < 14) {
      IDS();
      if(w == 7) wait_flags(ws, FLAG(t, layer, FL_O, li), tag);
      const rsrc_t rx = layer == 0 ? __builtin_amdgcn_make_buffer_rsrc((void *)x, 0, S * D * 2, 0x00020000) : rws;
      const v4u xr = ld16(rx, layer == 0 ? (16 * tt + li) * (D * 2) + g * 16 : lane * 16,
          layer == 0 ? slot * 64 : PB(t, 2) + FO(F_X2 + w * 16 + slot));
      BARRIER();
      STAMP(12 + layer * 30);
      v4u of[16];
      const int ob = opq(PB(t, layer) + FO(F_O + w * 16));
#pragma unroll
      for(int h = 0; h < 8; ++h) of[h] = ld16(rws, lane * 16 + (h & 3) * 1024, ob + X4(h));
      SB();
      v4u wf[32];
#pragma unroll
      for(int i = 0; i < 8; ++i) wf[i] = *(LAS v4u *)(L + L_WO + i * 1024 + lane * 16);
      SB();
      f32x4 a0 = {0.f, 0.f, 0.f, 0.f}, a1 = {0.f, 0.f, 0.f, 0.f};
#pragma unroll
      for(int h = 0; h < 16; ++h) {
        if(h < 8) of[h + 8] = ld16(rws, lane * 16 + (h & 3) * 1024, ob + X4(h + 8));
        if(h < 12)
          wf[2 * h + 8] = *(LAS v4u *)(L + L_WO + (2 * h + 8) * 1024 + lane * 16),
                     wf[2 * h + 9] = *(LAS v4u *)(L + L_WO + (2 * h + 9) * 1024 + lane * 16);
        a0 = mfma32(wf[2 * h], of[h], a0), a1 = mfma32(wf[2 * h + 1], of[h], a1);
        SB();
      }
      const v4u y = {pk2(a0[0] + bflo(xr.x), a0[1] + bfhi(xr.x)), pk2(a0[2] + bflo(xr.y), a0[3] + bfhi(xr.y)),
          pk2(a1[0] + bflo(xr.z), a1[1] + bfhi(xr.z)), pk2(a1[2] + bflo(xr.w), a1[3] + bfhi(xr.w))};
      st16(y, rws, lane * 16, PB(t, layer) + FO(F_X1 + w * 16 + slot));
      st8(piece_stats(y), rws, lane < 16 ? lane * 8 : OOB, PB(t, 2) + FO(F_ST + (layer * 8 + w) * 2) + slot * 128);
      STAMP(13 + layer * 30);
      DRAIN();
      BARRIER();
      if(w == 7 && lane == 0) set_word(ws, FLAG(t, layer, FL_X1, slot), tag);
    }
    STAMP(14 + layer * 30);
    {
      IDS();
      const int xb = opq(PB(t, layer) + FO(F_X1 + w * 16));
      if(w == 7) wait_flags(ws, FLAG(t, layer, FL_X1, (li < 14 ? li : 0)), tag);
      if(layer == 0 && ti < 112) {
        const int nt = ti >> 4, i = ti & 15;
        float sum = 0.f;
#pragma unroll
        for(int ks = 0; ks < 14; ++ks)
          sum += __uint_as_float(__builtin_amdgcn_raw_buffer_load_b32(rws, OFF_CSP + (ID_W1 + slot * 98 + ks * 7) * 64 + ti * 4, 0, AUX_SC1));
        *(LAS float *)(L + LC_CS1 + ti * 4) = sum;
        const int col = nt < 6 ? 112 * slot + 32 * (nt >> 1) + 8 * (i >> 2) + (i & 3) + 4 * (nt & 1) : 112 * slot + 96 + i;
        *(LAS float *)(L + LC_B1 + ti * 4) = bflo(b1[col]);
      }
      BARRIER();
      STAMP(15 + layer * 30);
      v4u xf[14];
      v2u sp[4];
      ROW_STATS(layer);
#pragma unroll
      for(int ks = 0; ks < XRING; ++ks) XLOAD(ks);
      SB();
      WT_DMA(ID_W1 + slot * 98, W1_EARLY, 98, w, 8, L_X);
      f32x4 acc[7];
#pragma unroll
      for(int n = 0; n < 7; ++n) acc[n] = f32x4{0.f, 0.f, 0.f, 0.f};
      float rs, nmr;
      const int wb = opq(ID_W2 + CSL(slot) * 224 + w * 28);
      SB();
#define STEP_B(ks) \
  if((ks) == 3) {   \
    SUM_STATS(); \
    const float mu = s1 * (1.f / D), var = fmaxf(s2 * (1.f / D) - mu * mu, 0.f), r1 = __builtin_amdgcn_rsqf(var + EPS); \
    rs = r1 * __builtin_amdgcn_rsqf(var * r1 * r1 + EPS), nmr = -mu * rs; \
    asm volatile("" : "+v"(rs), "+v"(nmr)); \
  } \
  STEP_X(ks); \
  if(layer == 0 && (ks) >= 6) { \
    _Pragma("unroll") for(int j = 8 + ((ks)-6) * 3; j < 8 + ((ks)-5) * 3 && j < 28; ++j) W2LOAD(j); \
  }
      GEMM_PART(7, 0, 7, 0, 9, 7, STEP_B);
      asm volatile("s_waitcnt vmcnt(%0)" ::"i"(14 - XRING) : "memory");
      BARRIER();
      GEMM_PART(7, 0, 7, 9, 14, 7, STEP_B);
      STAMP(16 + layer * 30);
      const int ho = PB(t, 0) + FO(F_H + w * 64 + (slot >> 1) * 8);
      f32x4 cc[7], bb[7];
#pragma unroll
      for(int n = 0; n < 7; ++n) cc[n] = *(LAS f32x4 *)(L + LC_CS1 + n * 64 + g * 16), bb[n] = *(LAS f32x4 *)(L + LC_B1 + n * 64 + g * 16);
      SB();
      unsigned hp[14];
#pragma unroll
      for(int n = 0; n < 7; ++n) {
        const f32x4 v = acc[n] * rs + (cc[n] * nmr + bb[n]);
        const f32x4 a = v * (v * v * -0.1029432396f + -2.3022081986f);
        const f32x4 e = {__builtin_amdgcn_exp2f(a[0]), __builtin_amdgcn_exp2f(a[1]), __builtin_amdgcn_exp2f(a[2]), __builtin_amdgcn_exp2f(a[3])};
        const f32x4 d = e + 1.f;
        const f32x4 r = {__builtin_amdgcn_rcpf(d[0]), __builtin_amdgcn_rcpf(d[1]), __builtin_amdgcn_rcpf(d[2]), __builtin_amdgcn_rcpf(d[3])};
        const f32x4 hv = v * r;
        hp[2 * n] = pk2(hv[0], hv[1]), hp[2 * n + 1] = pk2(hv[2], hv[3]);
        if(n & 1)
          st16(v4u{hp[2 * n - 2], hp[2 * n - 1], hp[2 * n], hp[2 * n + 1]}, rws, lane * 16,
              ho + (n < 4 ? 2 * (slot & 1) + (n >> 1) : 4 + (slot & 1)) * 1024);
        if(n == 6) st8(v2u{hp[12], hp[13]}, rws, lane * 16, ho + 6144 + (slot & 1) * 8);
      }
      STAMP(17 + layer * 30);
      DRAIN();
      BARRIER();
      STAMP(18 + layer * 30);
      if(w == 7) {
        if(lane == 0) set_word(ws, FLAG(t, layer, FL_H, slot), tag);
        if(slot < 14) wait_flags(ws, FLAG(t, layer, FL_H, li), tag);
      } else if(layer == 0 && slot < 14) {
        WT_DMA(ID_WQ + slot * 84, 0, WQ_HOP, w, 7, L_X);
      }
    }
    if(SLOT_OF(b0) < 14) {
      IDS();
      const int csl = CSL(slot), chalf = slot >= 7;
      BARRIER();
      STAMP(19 + layer * 30);
      const int ho = opq(PB(t, 0) + FO(F_H + 4 * chalf * 64 + w * 8));
      v4u hf[8];
#pragma unroll
      for(int i = 0; i < 8; ++i) hf[i] = ld16(rws, lane * 16 + (i >> 2) * 1024, ho + (i & 3) * FO(64));
      const int ow = 4 * chalf + (w >> 1), oks = 2 * csl + (w & 1);
      v4u xr, bb;
      SB();
      f32x4 acc[4][4];
#pragma unroll
      for(int q = 0; q < 4; ++q)
#pragma unroll
        for(int n = 0; n < 4; ++n) acc[q][n] = f32x4{0.f, 0.f, 0.f, 0.f};
#pragma unroll
      for(int k = 0; k < 7; ++k) {
#pragma unroll
        for(int q = 0; q < 4; ++q) {
          const v4u cur = hf[(k & 1) * 4 + q];
          if(k + 2 < 7) hf[(k & 1) * 4 + q] = ld16(rws, lane * 16 + (k & 3) * 1024, ho + q * FO(64) + 2048 + (k >> 2) * 4096);
          if(k == 5 && q == 1) xr = ld16(rws, lane * 16, opq(PB(t, layer) + FO(F_X1 + ow * 16 + oks)));
          if(k == 5 && q == 2)
            bb = __builtin_amdgcn_raw_buffer_load_b128(__builtin_amdgcn_make_buffer_rsrc((void *)b2, 0, D * 2, 0x00020000), g * 16, opq(oks * 64), 0);
#pragma unroll
          for(int n = 0; n < 4; ++n) acc[q][n] = mfma32(w2f[k * 4 + n], cur, acc[q][n]);
          SB();
        }
      }
      STAMP(20 + layer * 30);
      if(layer == 0) WT_DMA(ID_WQ + slot * 84, WQ_HOP, 84, w, 8, L_X);
      f32x4 a0, a1;
#pragma unroll
      for(int f = 0; f < 8; ++f) {
        const f32x4 p0 = acc[f >> 1][2 * (f & 1)], p1 = acc[f >> 1][2 * (f & 1) + 1];
        if(f == w) a0 = p0, a1 = p1;
        else
          *(LAS v4u *)(L + L_RED + (f * 7 + (w < f ? w : w - 1)) * 1024 + lane * 16) =
              v4u{pk2(p0[0], p0[1]), pk2(p0[2], p0[3]), pk2(p1[0], p1[1]), pk2(p1[2], p1[3])};
      }
      BARRIER();
      STAMP(21 + layer * 30);
      {
        const unsigned one = g == (li >> 2) ? ((li & 1) ? 0x3F800000u : 0x00003F80u) : 0u;
        const v4u e0 = {(li & 2) ? 0u : one, (li & 2) ? one : 0u, 0u, 0u}, e1 = {0u, 0u, (li & 2) ? 0u : one, (li & 2) ? one : 0u};
        v4u p[7];
#pragma unroll
        for(int s = 0; s < 7; ++s) p[s] = *(LAS v4u *)(L + L_RED + (w * 7 + s) * 1024 + lane * 16);
        a0 = mfma32(e0, xr, a0), a1 = mfma32(e1, xr, a1);
        a0 = mfma32(e0, bb, a0), a1 = mfma32(e1, bb, a1);
#pragma unroll
        for(int s = 0; s < 7; ++s) a0 = mfma32(e0, p[s], a0), a1 = mfma32(e1, p[s], a1);
      }
      const v4u y = {pk2(a0[0], a0[1]), pk2(a0[2], a0[3]), pk2(a1[0], a1[1]), pk2(a1[2], a1[3])};
      if(layer == 0) {
        st16(y, rws, lane * 16, PB(t, 2) + FO(F_X2 + ow * 16 + oks));
        st8(piece_stats(y), rws, lane < 16 ? lane * 8 : OOB, PB(t, 2) + FO(F_ST + (16 + ow) * 2) + oks * 128);
        STAMP(22 + layer * 30);
      } else {
        *(GAS v4u *)(out + (16 * (8 * t + ow) + li) * D + 32 * oks + 8 * g) = y;
      }
    }
    STAMP(23 + layer * 30);
    if(layer == 0) {
      IDS();
      if(slot >= 14) WT_DMA(ID_WQ + slot * 84, 0, 84, w, 8, L_X);
      DRAIN();
      BARRIER();
      if(w == 7) {
        if(lane == 0 && slot < 14) set_word(ws, FLAG(t, 0, FL_X2, slot), tag);
        wait_flags(ws, FLAG(t, 0, FL_X2, (li < 14 ? li : 0)), tag);
      }
      BARRIER();
    }
  };
  run_layer(IntC<0>{});
  run_layer(IntC<1>{});
  STAMP(61);
}
}

extern "C" void kernel_launch(void *const *d_in, const int *in_sizes, int n_in, void *d_out, int out_size, void *d_ws, size_t ws_size,
    hipStream_t stream) {
  const size_t pad = (size_t)(-(intptr_t)d_ws) & (size_t)0x1FFFFF;
  fused_kernel<<<NWG, NTHR, 0, stream>>>((const u16 *)d_in[0], (const u16 *)d_in[1], (const u16 *)d_in[2], (const u16 *)d_in[4],
      (const u16 *)d_in[5], (const u16 *)d_in[6], (const u16 *)d_in[7], (u16 *)d_out, (unsigned char *)d_ws + pad);
}
```

```cpp
#include <hip/hip_runtime.h>
#include <stdint.h>

#define GAS __attribute__((address_space(1)))
#define LAS __attribute__((address_space(3)))
#define DI __device__ __forceinline__
typedef unsigned v4u __attribute__((ext_vector_type(4)));
typedef unsigned v2u __attribute__((ext_vector_type(2)));
typedef float f32x4 __attribute__((ext_vector_type(4)));
typedef float f32x2 __attribute__((ext_vector_type(2)));
typedef __bf16 bf16x8 __attribute__((ext_vector_type(8)));
typedef __bf16 bf16x2 __attribute__((ext_vector_type(2)));
typedef short s16x4 __attribute__((ext_vector_type(4)));
typedef unsigned long long u64;
typedef unsigned short u16;
typedef __amdgpu_buffer_rsrc_t rsrc_t;

#define TILE_OF(b) ((((b)&7) >> 1) * 4 + ((b) >> 6))
#define SLOT_OF(b) (((b)&1) * 8 + (((b) >> 3) & 7))
#define CS14(s) ((s) >= 14 ? 0 : (s))
#define CSL(s) ((s) >= 14 ? 0 : (s) >= 7 ? (s)-7 : (s))

namespace {
template <int V> struct IntC {
  static constexpr int value = V;
};
constexpr int S = 2048, D = 448, F = 1792;
constexpr int NWG = 256, NTHR = 512;
constexpr float EPS = 1e-5f;
constexpr int AUX_SC1 = 16;
constexpr unsigned SPIN_CAP = 1u << 16;
constexpr int OOB = 0x7F000000;
constexpr int XRING = 8;
constexpr int WDEP = 7;
constexpr u64 VOTE = 1ull << 63;

constexpr int FS = 128;
constexpr int OFF_EPOCH = 0, OFF_PREPG = 4096, OFF_PREPF = 8192, OFF_SEL = 49152;
constexpr int OFF_CSP = 1 << 20;
constexpr int OFF_WT = 2 << 20;
constexpr int ID_WO = 0, ID_W1 = 448, ID_W2 = 2016, ID_WQ = 3584, ID_END = 4928;
constexpr int OFF_PROBE = 12 << 20, OFF_STAMP = 14 << 20;
constexpr int PG0 = 8, WS_BYTES = (PG0 + 48) << 21;
constexpr int F_KV = 0, F_O = 256, F_X1 = 384, F_H = 512, F_X2 = 0, F_ST = 128, F_FL = 192;
constexpr int FL_KV = 0, FL_O = 1, FL_X1 = 2, FL_H = 3, FL_X2 = 4;
#define FO(f) ((((f) >> 3) << 14) + (((f)&7) << 10))
#define PB(t_, p_) (((PG0 + 3 * (t_) + (p_)) << 21) + (((side ^ (t_) ^ (p_) ^ ((t_) >> 3)) & 1) << 13))
#define FLAG(t_, layer_, ph_, i_) (PB(t_, 2) + FO(F_FL + ((layer_)*5 + (ph_)) * 2) + (i_)*FS)
#define WT(f) (OFF_WT + (f)*1024)
static_assert(OFF_CSP + ID_END * 64 <= OFF_WT && OFF_WT + ID_END * 1024 <= OFF_PROBE && (PG0 & 1) == 0, "map");

constexpr int L_X = 0;
constexpr int L_KV = 100352 - 32768;
constexpr int L_WO = 100352 + 16384;
constexpr int L_RED = 86016;
constexpr int L_C = L_WO + 32768;
constexpr int LC_CSQ = L_C, LC_CS1 = L_C + 512, LC_B1 = L_C + 1024;
constexpr int LS_Q = L_C + 1536, LS_HOLD = LS_Q + 4, LS_KV = LS_Q + 8, LS_SIDE = LS_Q + 32;
constexpr int LDS_BYTES = L_C + 2048;
constexpr int W1_EARLY = 63;
constexpr int WQ_HOP = 28;
static_assert(W1_EARLY * 1024 <= L_KV && L_RED + 56 * 1024 <= L_C, "LDS map");

DI f32x4 mfma32(v4u a, v4u b, f32x4 c) {
  return __builtin_amdgcn_mfma_f32_16x16x32_bf16(__builtin_bit_cast(bf16x8, a), __builtin_bit_cast(bf16x8, b), c, 0, 0, 0);
}
DI f32x4 mfma16(v2u a, v2u b, f32x4 c) {
  return __builtin_amdgcn_mfma_f32_16x16x16bf16_1k(__builtin_bit_cast(s16x4, a), __builtin_bit_cast(s16x4, b), c, 0, 0, 0);
}
DI unsigned pk2(float a, float b) {
  f32x2 v = {a, b};
  return __builtin_bit_cast(unsigned, __builtin_convertvector(v, bf16x2));
}
DI float bflo(unsigned u) { return __uint_as_float(u << 16); }
DI float bfhi(unsigned u) { return __uint_as_float(u & 0xFFFF0000u); }
DI float dot2(unsigned a, unsigned b, float c) {
  return __builtin_amdgcn_fdot2_f32_bf16(__builtin_bit_cast(bf16x2, a), __builtin_bit_cast(bf16x2, b), c, false);
}
DI float gsum(float x) {
  auto r = __builtin_amdgcn_permlane32_swap(__float_as_uint(x), __float_as_uint(x), false, false);
  x = __uint_as_float(r[0]) + __uint_as_float(r[1]);
  auto q = __builtin_amdgcn_permlane16_swap(__float_as_uint(x), __float_as_uint(x), false, false);
  return __uint_as_float(q[0]) + __uint_as_float(q[1]);
}
DI float gmax(float x) {
  auto r = __builtin_amdgcn_permlane32_swap(__float_as_uint(x), __float_as_uint(x), false, false);
  x = fmaxf(fmaxf(x, __uint_as_float(r[0])), __uint_as_float(r[1]));
  auto q = __builtin_amdgcn_permlane16_swap(__float_as_uint(x), __float_as_uint(x), false, false);
  return fmaxf(fmaxf(x, __uint_as_float(q[0])), __uint_as_float(q[1]));
}
DI v4u ld16(rsrc_t rs, int voff, int soff) { return __builtin_amdgcn_raw_buffer_load_b128(rs, voff, soff, AUX_SC1); }
DI v2u ld8(rsrc_t rs, int voff, int soff) { return __builtin_amdgcn_raw_buffer_load_b64(rs, voff, soff, AUX_SC1); }
DI void st16(v4u v, rsrc_t rs, int voff, int soff) {
  __builtin_amdgcn_raw_buffer_store_b128(v, rs, voff, soff, AUX_SC1);
  asm volatile("s_nop 1" ::"v"(v) : "memory");
}
DI void st8(v2u v, rsrc_t rs, int voff, int soff) { __builtin_amdgcn_raw_buffer_store_b64(v, rs, voff, soff, AUX_SC1); }
DI int opq(int v) {
  asm volatile("" : "+s"(v));
  return v;
}
DI int opqv(int v) {
  asm volatile("" : "+v"(v));
  return v;
}
DI v2u trread(LAS unsigned char *p) { return __builtin_bit_cast(v2u, __builtin_amdgcn_ds_read_tr16_b64_v4i16((LAS s16x4 *)p)); }
#define DRAIN() asm volatile("s_waitcnt vmcnt(0)" ::: "memory")
#define BARRIER() asm volatile("s_waitcnt lgkmcnt(0)\n\ts_barrier" ::: "memory")
#define SB() __builtin_amdgcn_sched_barrier(0)
#define dma1k(rs, voff, soff, ldsaddr) \
  asm volatile("s_mov_b32 m0, %0\n\ts_nop 0\n\tbuffer_load_dwordx4 %1, %2, %3 offen sc1 lds" ::"s"((unsigned)(ldsaddr)), "v"((int)(voff)), \
               "s"(rs), "s"((int)(soff)) \
               : "memory")
#define dma1k_input(rs, voff, soff, ldsaddr) \
  asm volatile("s_mov_b32 m0, %0\n\ts_nop 0\n\tbuffer_load_dwordx4 %1, %2, %3 offen lds" ::"s"((unsigned)(ldsaddr)), "v"((int)(voff)), \
               "s"(rs), "s"((int)(soff)) \
               : "memory")
DI void wait_flags(const unsigned char *ws, int off, u64 tag) {
  const GAS u64 *p = (const GAS u64 *)(ws + off);
  for(unsigned spin = 0; spin < SPIN_CAP; ++spin) {
    const u64 v = __hip_atomic_load(p, __ATOMIC_RELAXED, __HIP_MEMORY_SCOPE_AGENT);
    if(__all(v == tag)) break;
    __builtin_amdgcn_s_sleep(1);
  }
  __builtin_amdgcn_fence(__ATOMIC_ACQUIRE, "wavefront");
}
DI void set_word(unsigned char *ws, int off, u64 v) {
  __hip_atomic_store((GAS u64 *)(ws + off), v, __ATOMIC_RELAXED, __HIP_MEMORY_SCOPE_AGENT);
}
DI unsigned lds_count(LAS unsigned char *p, unsigned add, int lane) {
  unsigned old = 0;
  if(lane == 0) old = __hip_atomic_fetch_add((LAS unsigned *)p, add, __ATOMIC_RELAXED, __HIP_MEMORY_SCOPE_WORKGROUP);
  return __builtin_amdgcn_readfirstlane(old);
}
DI v2u piece_stats(v4u y) {
  float s1 = 0.f, s2 = 0.f;
#pragma unroll
  for(int j = 0; j < 4; ++j) s1 = dot2(y[j], 0x3F803F80u, s1), s2 = dot2(y[j], y[j], s2);
  return v2u{__float_as_uint(gsum(s1)), __float_as_uint(gsum(s2))};
}

#define STAMP(i)

__global__ void __launch_bounds__(NTHR, 2)
    fused_kernel(const u16 *__restrict__ x, const u16 *__restrict__ wqkv, const u16 *__restrict__ wo, const u16 *__restrict__ w1,
        const u16 *__restrict__ b1, const u16 *__restrict__ w2, const u16 *__restrict__ b2, u16 *__restrict__ out, unsigned char *ws) {
  __shared__ __attribute__((aligned(16))) unsigned char smem[LDS_BYTES];
  LAS unsigned char *L = (LAS unsigned char *)smem;
  const unsigned ldsb = (unsigned)(size_t)L;
  const int tid = threadIdx.x, w0 = __builtin_amdgcn_readfirstlane(tid >> 6), b0 = blockIdx.x;
#define IDS() \
  const int ti = opqv(tid), lane = ti & 63, w = opq(w0), b = opq(b0), t = TILE_OF(b), slot = SLOT_OF(b); \
  const int tt = 8 * t + w, g = lane >> 4, li = lane & 15; \
  (void)g, (void)li, (void)tt, (void)slot
#define WT_DMA(id0, from, to, w_, nw_, ldsoff) \
  for(int i_ = (from) + (w_); i_ < (to); i_ += (nw_)) dma1k(rws, lane * 16, WT((id0) + i_), ldsb + (ldsoff) + i_ * 1024)
#define GEMM_PART(NT, N0, NN, K0, K1, SWAPN, STEP) \
  { \
    constexpr int CNT = ((K1) - (K0)) * (NN); \
    v4u wr[WDEP]; \
    _Pragma("unroll") for(int i = 0; i < WDEP; ++i) wr[i] = *(LAS v4u *)(L + L_X + (((K0) + i / (NN)) * (NT) + (N0) + i % (NN)) * 1024 + lane * 16); \
    SB(); \
    _Pragma("unroll") for(int i = 0; i < CNT; ++i) { \
      const int ks = (K0) + i / (NN), n = (N0) + i % (NN); \
      if(i % (NN) == 0) { \
        STEP(ks); \
      } \
      const v4u cur = wr[i % WDEP]; \
      if(i + WDEP < CNT) \
        wr[i % WDEP] = *(LAS v4u *)(L + L_X + (((K0) + (i + WDEP) / (NN)) * (NT) + (N0) + (i + WDEP) % (NN)) * 1024 + lane * 16); \
      acc[n] = n < (SWAPN) ? mfma32(cur, xf[ks], acc[n]) : mfma32(xf[ks], cur, acc[n]); \
      SB(); \
    } \
  }
#define X4(k) ((((k) >> 3) << 14) + ((((k) >> 2) & 1) << 12))
#define XLOAD(k) xf[k] = ld16(rws, lane * 16 + ((k)&3) * 1024, xb + X4(k))
#define STEP_NONE(ks)
#define STEP_X(ks) \
  if((ks) + XRING < 14) XLOAD((ks) + XRING)
#define ROW_STATS(which) \
  _Pragma("unroll") for(int i = 0; i < 4; ++i) \
      sp[i] = ld8(rws, (i == 3 && g > 1) ? OOB : li * 8 + g * 128 + i * 512, PB(t, 2) + FO(F_ST + ((which)*8 + w) * 2))
#define SUM_STATS() \
  const float s1 = gsum((__uint_as_float(sp[0].x) + __uint_as_float(sp[1].x)) + (__uint_as_float(sp[2].x) + __uint_as_float(sp[3].x))); \
  const float s2 = gsum((__uint_as_float(sp[0].y) + __uint_as_float(sp[1].y)) + (__uint_as_float(sp[2].y) + __uint_as_float(sp[3].y)))
#define W2LOAD(j) w2f[j] = ld16(rws, lane * 16 + ((j)&3) * 1024, WT(wb + ((j) & ~3)))
  const rsrc_t rws = __builtin_amdgcn_make_buffer_rsrc((void *)ws, 0, WS_BYTES, 0x00020000);
  STAMP(0);
  u64 tag = 0;
  int side = 0;
  v4u w2f[28];

  auto run_layer = [&](auto LC) __attribute__((always_inline)) {
    constexpr int layer = decltype(LC)::value;
    v4u qf;
    {
      IDS();
      v4u xf[14];
      float mu, rs, mur[4], rsr[4];
      f32x4 acc[6];
#pragma unroll
      for(int n = 0; n < 6; ++n) acc[n] = f32x4{0.f, 0.f, 0.f, 0.f};
#define A_FINISH_STATS() \
  mu = s1 * (1.f / D), rs = __builtin_amdgcn_rsqf(fmaxf(s2 * (1.f / D) - mu * mu, 0.f) + EPS); \
  _Pragma("unroll") for(int r = 0; r < 4; ++r) mur[r] = __shfl(mu, 4 * g + r), rsr[r] = __shfl(rs, 4 * g + r); \
  asm volatile("" : "+v"(mu), "+v"(rs), "+v"(mur[0]), "+v"(mur[1]), "+v"(mur[2]), "+v"(mur[3]), "+v"(rsr[0]), "+v"(rsr[1]), "+v"(rsr[2]), "+v"(rsr[3]))
      if(layer == 0) {
        int dst[2], dst1[2], tro[2], tre[2];
        unsigned char *pp = ws + OFF_PROBE + (b & 31) * 16384 + w * 512;
        if(w < 3 && b >= 224) {
          asm volatile("global_store_dword %0, %1, off sc1" ::"v"(pp + (lane & 1) * 8192 + ((lane >> 1) & 1) * 128), "v"(0u) : "memory");
        } else if(w < 3) {
          const int r = lane >> 2, c = lane & 3, head = b / 14, ks = b - 14 * head;
          const rsrc_t rq = __builtin_amdgcn_make_buffer_rsrc((void *)wqkv, 0, D * 3 * D * 2, 0x00020000);
          const int o0 = (32 * ks + r) * 2688 + w * 896 + 56 * head + c * 16;
          dma1k_input(rq, o0, 0, ldsb + L_WO + w * 2048);
          dma1k_input(rq, o0, 16 * 2688, ldsb + L_WO + w * 2048 + 1024);
          dst[0] = ID_WQ + head * 84 + ks * 6 + w * 2, dst1[0] = dst[0] + 1, tro[0] = 16, tre[0] = 8;
        }
        v2u ep;
        asm volatile("buffer_load_dwordx2 %0, off, %1, 0 sc1" : "=v"(ep) : "s"(rws) : "memory");
        if(ti < 8) *(LAS unsigned *)(L + LS_Q + 4 * ti) = 0u;
        const rsrc_t rx = __builtin_amdgcn_make_buffer_rsrc((void *)x, 0, S * D * 2, 0x00020000);
        const int xvo = (16 * tt + (lane >> 3)) * (D * 2) + (((lane & 7) ^ (lane >> 3)) * 16);
#define X0_DMA() \
  _Pragma("unroll") for(int i = 0; i < 14; ++i) dma1k_input(rx, xvo, (i & 1) * 8 * (D * 2) + (i >> 1) * 128, ldsb + w * 14336 + i * 1024)
#define TAKE_TAG() \
  asm volatile("s_waitcnt vmcnt(0)" : "+v"(ep)::"memory"); \
  tag = (((u64)(unsigned)__builtin_amdgcn_readfirstlane(ep.y) << 32) | (unsigned)__builtin_amdgcn_readfirstlane(ep.x)) + 1ull
#define PREP_OUT(rd, QPAD) \
  { \
    LAS unsigned char *p = L + L_WO + ((rd)*8 + w) * 2048 + (8 * g + (li >> 2)) * 64 + (li & 3) * tro[rd]; \
    const v2u a0 = trread(p), a1 = trread(p + 256), c0 = trread(p + tre[rd]), c1 = trread(p + tre[rd] + 256); \
    const v4u f0 = {a0.x, a0.y, a1.x, a1.y}; \
    v4u f1 = {c0.x, c0.y, c1.x, c1.y}; \
    if(QPAD && li >= 12) f1 = v4u{0u, 0u, 0u, 0u}; \
    const bool ok = dst[rd] < ID_END; \
    st16(f0, rws, ok ? lane * 16 : OOB, WT(dst[rd])); \
    st16(f1, rws, ok ? lane * 16 : OOB, WT(dst1[rd])); \
    float ca = 0.f, cb = 0.f; \
    _Pragma("unroll") for(int j = 0; j < 4; ++j) ca = dot2(f0[j], 0x3F803F80u, ca), cb = dot2(f1[j], 0x3F803F80u, cb); \
    ca = gsum(ca), cb = gsum(cb); \
    __builtin_amdgcn_raw_buffer_store_b32( \
        __float_as_uint(g ? cb : ca), rws, (ok && g < 2) ? OFF_CSP + (g ? dst1[rd] : dst[rd]) * 64 + li * 4 : OOB, 0, AUX_SC1); \
  }
        BARRIER();
        if(w < 3) {
          unsigned vote = 1u;
          TAKE_TAG();
          STAMP(25);
          if(b >= 224) {
            unsigned v;
            unsigned best[2] = {~0u, ~0u};
#pragma unroll
            for(int i = 0; i < 4; ++i) {
              u64 t0, t1;
              asm volatile(
                  "s_memtime %0\n\ts_waitcnt lgkmcnt(0)\n\tglobal_load_dword %2, %3, off sc1\n\ts_waitcnt vmcnt(0)\n\ts_memtime %1\n\ts_waitcnt lgkmcnt(0)"
                  : "=&s"(t0), "=&s"(t1), "=&v"(v)
                  : "v"(pp + ((i ^ w ^ (b >> 3)) & 1) * 8192 + (i >> 1) * 128)
                  : "memory");
              const unsigned dt = (unsigned)(t1 - t0);
              if((i ^ w ^ (b >> 3)) & 1) best[1] = min(best[1], dt);
              else best[0] = min(best[0], dt);
            }
            if((best[1] < best[0]) != (bool)((b >> 2) & 1)) vote = 0x101u;
          } else {
            PREP_OUT(0, true);
          }
          lds_count(L + LS_HOLD, 1u, lane);
          DRAIN();
          STAMP(26);
          const unsigned cnt = lds_count(L + LS_Q, vote, lane) + vote;
          if((cnt & 0xFFu) == 3u && lane == 0) set_word(ws, OFF_PREPF + b * FS, cnt >= 0x200u ? tag ^ VOTE : tag);
          X0_DMA();
          DRAIN();
        } else {
#pragma unroll
          for(int rd = 0; rd < 2; ++rd) {
            const int id = rd == 0 ? b * 5 + (w - 3) : w < 5 ? 1280 + b * 2 + (w - 3) : 1792, r = lane >> 2, c = lane & 3;
            const u16 *src;
            int bytes, o0, o1;
            tro[rd] = 16, tre[rd] = 8;
            if(id < 224) {
              const int ks = id / 14, sl = id - ks * 14;
              src = wo, bytes = D * D * 2;
              o0 = (28 * ks + r) * 896 + 64 * sl + c * 16, o1 = r < 12 ? o0 + 16 * 896 : OOB;
              dst[rd] = ID_WO + sl * 32 + ks * 2, dst1[rd] = dst[rd] + 1;
            } else if(id < 224 + 672) {
              const int i2 = id - 224, ks = i2 / 48, rem = i2 - ks * 48, sl = rem / 3, p = rem - sl * 3;
              src = w1, bytes = D * F * 2;
              o0 = (32 * ks + r) * 3584 + 224 * sl + 64 * p + c * 16, o1 = o0 + 16 * 3584;
              dst[rd] = ID_W1 + sl * 98 + ks * 7 + 2 * p, dst1[rd] = dst[rd] + 1;
            } else if(id < 896 + 112) {
              const int i2 = id - 896, ks = i2 >> 3, sl = 2 * (i2 & 7);
              src = w1, bytes = D * F * 2;
              o0 = (32 * ks + r) * 3584 + 224 * (sl + (c >> 1)) + 192 + (c & 1) * 16, o1 = o0 + 16 * 3584;
              dst[rd] = ID_W1 + sl * 98 + ks * 7 + 6, dst1[rd] = dst[rd] + 98, tro[rd] = 8, tre[rd] = 32;
            } else {
              const int i2 = id - 1008, hks = i2 / 14, cb = i2 - hks * 14, wv = hks / 7, kk = hks - 7 * wv;
              src = w2, bytes = F * D * 2;
              const int row = 224 * wv +
                  (kk < 4 ? 112 * (kk >> 1) + 32 * (kk & 1) + r : kk < 6 ? 112 * (kk - 4) + 64 + r : 96 + 112 * ((r >> 2) & 1) + 4 * (r >> 3) + (r & 3));
              o0 = row * 896 + 64 * cb + c * 16, o1 = o0 + (kk < 6 ? 16 : 8) * 896;
              dst[rd] = ID_W2 + (cb >> 1) * 224 + hks * 4 + (cb & 1) * 2, dst1[rd] = dst[rd] + 1;
              if(id >= 1792) o0 = o1 = OOB, dst[rd] = dst1[rd] = ID_END;
            }
            const rsrc_t rsw = __builtin_amdgcn_make_buffer_rsrc((void *)src, 0, bytes, 0x00020000);
            asm volatile("s_nop 4" ::: "memory");
            dma1k_input(rsw, o0, 0, ldsb + L_WO + (rd * 8 + w) * 2048);
            dma1k_input(rsw, o1, 0, ldsb + L_WO + (rd * 8 + w) * 2048 + 1024);
          }
          for(unsigned spin = 0; spin < (1u << 20); ++spin)
            if(__builtin_amdgcn_readfirstlane(__hip_atomic_load((LAS unsigned *)(L + LS_HOLD), __ATOMIC_RELAXED, __HIP_MEMORY_SCOPE_WORKGROUP)) >= 3)
              break;
          X0_DMA();
          STAMP(25);
          TAKE_TAG();
          STAMP(26);
          if(w == 7) wait_flags(ws, OFF_PREPF + (slot * 14 + (li < 14 ? li : 0)) * FS, tag);
        }
        STAMP(27);
#pragma unroll
        for(int ks = 0; ks < 14; ++ks)
          xf[ks] = *(LAS v4u *)(L + w * 14336 + ((ks >> 1) * 2 + (li >> 3)) * 1024 + (li & 7) * 128 + (((ks & 1) * 4 + g) ^ (li & 7)) * 16);
        BARRIER();
        STAMP(2);
        WT_DMA(ID_WQ + slot * 84, 0, 84, w, 8, L_X);
        {
          float s1 = 0.f, s2 = 0.f, s3 = 0.f, s4 = 0.f;
#pragma unroll
          for(int ks = 0; ks < 14; ++ks) {
            s1 = dot2(xf[ks][0], 0x3F803F80u, s1), s2 = dot2(xf[ks][0], xf[ks][0], s2);
            s3 = dot2(xf[ks][1], 0x3F803F80u, s3), s4 = dot2(xf[ks][1], xf[ks][1], s4);
            s1 = dot2(xf[ks][2], 0x3F803F80u, s1), s2 = dot2(xf[ks][2], xf[ks][2], s2);
            s3 = dot2(xf[ks][3], 0x3F803F80u, s3), s4 = dot2(xf[ks][3], xf[ks][3], s4);
          }
          s1 = gsum(s1 + s3), s2 = gsum(s2 + s4);
          A_FINISH_STATS();
        }
        unsigned cv[14];
        if(w >= 3) {
          PREP_OUT(0, false);
          PREP_OUT(1, false);
          asm volatile("s_waitcnt vmcnt(11)" ::: "memory");
        } else {
          asm volatile("s_waitcnt vmcnt(5)" ::: "memory");
#pragma unroll
          for(int ks = 0; ks < 14; ++ks)
            cv[ks] = __builtin_amdgcn_raw_buffer_load_b32(rws, ti < 96 ? OFF_CSP + (ID_WQ + slot * 84 + ks * 6) * 64 + ti * 4 : OOB, 0, AUX_SC1);
          SB();
        }
        BARRIER();
        STAMP(3);
        GEMM_PART(6, 2, 4, 0, 7, 4, STEP_NONE);
        if(ti < 96) {
          float csum = 0.f;
#pragma unroll
          for(int ks = 0; ks < 14; ++ks) csum += __uint_as_float(cv[ks]);
          *(LAS float *)(L + LC_CSQ + ti * 4) = csum;
        }
        if(w == 2) {
          u64 v = 0;
          for(unsigned spin = 0; spin < SPIN_CAP; ++spin) {
            v = __hip_atomic_load((const GAS u64 *)(ws + OFF_PREPF + (224 + (lane & 31)) * FS), __ATOMIC_RELAXED, __HIP_MEMORY_SCOPE_AGENT);
            if(__all((v | VOTE) == (tag | VOTE))) break;
          }
          const int ones = __popcll(__ballot(v != tag));
          if(lane == 0) *(LAS int *)(L + LS_SIDE) = ones > 32;
        }
        DRAIN();
        BARRIER();
        side = __builtin_amdgcn_readfirstlane(*(LAS int *)(L + LS_SIDE));
        if(ti == 0) set_word(ws, OFF_PREPG + b * 8, tag);
        GEMM_PART(6, 2, 4, 7, 14, 4, STEP_NONE);
      } else {
        STAMP(31);
        const int xb = opq(PB(t, 2) + FO(F_X2 + w * 16));
        v2u sp[4];
        ROW_STATS(2);
#pragma unroll
        for(int ks = 0; ks < XRING; ++ks) XLOAD(ks);
        SB();
        STAMP(33);
#define STEP_XA(ks) \
  if((ks) == 3) { \
    SUM_STATS(); \
    A_FINISH_STATS(); \
  } \
  STEP_X(ks)
        GEMM_PART(6, 2, 4, 0, 14, 4, STEP_XA);
      }
      STAMP(4 + layer * 30);
      const float qs = rs * (0.18898223650461362f * 1.4426950408889634f);
      f32x4 c[4];
#pragma unroll
      for(int n = 0; n < 4; ++n) c[n] = *(LAS f32x4 *)(L + LC_CSQ + n * 64 + g * 16);
      const float c4 = *(LAS float *)(L + LC_CSQ + 256 + li * 4), c5 = *(LAS float *)(L + LC_CSQ + 320 + li * 4);
      f32x4 a0 = (acc[2] - mu * c[2]) * rs, a1 = (acc[3] - mu * c[3]) * rs;
      const v4u kf = {pk2(a0[0], a0[1]), pk2(a0[2], a0[3]), g == 3 ? 0u : pk2(a1[0], a1[1]), g == 3 ? 0u : pk2(a1[2], a1[3])};
#pragma unroll
      for(int r = 0; r < 4; ++r) a0[r] = (acc[4][r] - mur[r] * c4) * rsr[r], a1[r] = (acc[5][r] - mur[r] * c5) * rsr[r];
      const unsigned vpad = li == 12 ? 0x3F803F80u : 0u;
      const v4u vf = {pk2(a0[0], a0[1]), pk2(a0[2], a0[3]), li >= 12 ? vpad : pk2(a1[0], a1[1]), li >= 12 ? vpad : pk2(a1[2], a1[3])};
      const int kvo = PB(t, layer) + FO(F_KV + (slot * 8 + w) * 2);
      st16(kf, rws, lane * 16, kvo);
      st16(vf, rws, lane * 16 + 1024, kvo);
      *(LAS v4u *)(L + L_KV + (16 + w) * 2048 + lane * 16) = kf;
      *(LAS v4u *)(L + L_KV + (16 + w) * 2048 + 1024 + lane * 16) = vf;
      STAMP(5 + layer * 30);
#define STEP_KVF(ks) \
  if((ks) == 9) { \
    DRAIN(); \
    if(lds_count(L + LS_KV, 1u, lane) == 8u * (layer + 1) - 1u && lane == 0) set_word(ws, FLAG(t, layer, FL_KV, slot), tag); \
    SB(); \
  }
      GEMM_PART(6, 0, 2, 0, 14, 4, STEP_KVF);
      a0 = (acc[0] - mu * c[0]) * qs, a1 = (acc[1] - mu * c[1]) * qs;
      qf = v4u{pk2(a0[0], a0[1]), pk2(a0[2], a0[3]), g == 3 ? 0x00003F80u : pk2(a1[0], a1[1]), g == 3 ? 0u : pk2(a1[2], a1[3])};
    }
    BARRIER();
    {
      IDS();
      if(layer == 0 && w == 6) {
        for(unsigned spin = 0; spin < SPIN_CAP; ++spin) {
          const v4u p0 = ld16(rws, OFF_PREPG + lane * 32, 0), p1 = ld16(rws, OFF_PREPG + lane * 32 + 16, 0);
          const unsigned tl = (unsigned)tag, th = (unsigned)(tag >> 32);
          if(__all(p0.x == tl && p0.z == tl && p1.x == tl && p1.z == tl && p0.y == th && p0.w == th && p1.y == th && p1.w == th)) break;
          __builtin_amdgcn_s_sleep(1);
          asm volatile("" ::: "memory");
        }
        __builtin_amdgcn_fence(__ATOMIC_ACQUIRE, "wavefront");
        if(b == 0 && lane == 0) set_word(ws, OFF_EPOCH, tag);
      }
      STAMP(6 + layer * 30);
      if(w == 7 && t > 0) wait_flags(ws, ((lane & 1) && t > 1) ? FLAG(t - 2, layer, FL_KV, slot) : FLAG(t - 1, layer, FL_KV, slot), tag);
      BARRIER();
      STAMP(7 + layer * 30);
#pragma unroll
      for(int i = 0; i < 4; ++i) {
        const int pc = i * 8 + w;
        if(16 * t - 32 + pc >= 0) dma1k(rws, lane * 16, PB(t - 2 + (i >> 1), layer) + FO(F_KV + slot * 16 + (pc & 15)), ldsb + L_KV + pc * 1024);
        else *(LAS v4u *)(L + L_KV + pc * 1024 + lane * 16) = v4u{0u, 0u, (g == 3 && !(pc & 1)) ? 0x0000F149u : 0u, 0u};
      }
      f32x4 c0, c16;
#pragma unroll
      for(int r = 0; r < 4; ++r) c0[r] = (4 * g + r > li) ? 0.f : -1e30f, c16[r] = (4 * g + r <= li) ? 0.f : -1e30f;
      DRAIN();
      BARRIER();
      STAMP(8 + layer * 30);
      LAS unsigned char *kv = L + L_KV + w * 2048 + lane * 16;
      const int wb = opq(ID_W2 + CSL(slot) * 224 + w * 28);
      f32x4 s[17];
      {
        v4u kf[17];
#pragma unroll
        for(int i = 0; i < 6; ++i) kf[i] = *(LAS v4u *)(kv + i * 2048);
        SB();
#pragma unroll
        for(int i = 0; i < 17; ++i) {
          if(i + 6 < 17) kf[i + 6] = *(LAS v4u *)(kv + (i + 6) * 2048);
          s[i] = mfma32(kf[i], qf, i == 0 ? c0 : i == 16 ? c16 : f32x4{0.f, 0.f, 0.f, 0.f});
          if((i & 3) == 1 && i < 16)
            dma1k(rws, lane * 16, WT(ID_WO + CS14(slot) * 32 + (i >> 2) * 8 + w), ldsb + L_WO + ((i >> 2) * 8 + w) * 1024);
          SB();
        }
      }
      v4u vf[17];
#pragma unroll
      for(int i = 0; i < 4; ++i) vf[i] = *(LAS v4u *)(kv + i * 2048 + 1024);
      float mx[4] = {-1e30f, -1e30f, -1e30f, -1e30f};
#pragma unroll
      for(int i = 0; i < 17; ++i) {
        mx[(2 * i) & 3] = fmaxf(fmaxf(mx[(2 * i) & 3], s[i][0]), s[i][1]);
        mx[(2 * i + 1) & 3] = fmaxf(fmaxf(mx[(2 * i + 1) & 3], s[i][2]), s[i][3]);
      }
      const float m = gmax(fmaxf(fmaxf(fmaxf(mx[0], mx[1]), mx[2]), mx[3]));
      f32x4 o0 = {0.f, 0.f, 0.f, 0.f}, o1 = {0.f, 0.f, 0.f, 0.f};
#pragma unroll
      for(int i = 0; i < 17; ++i) {
        if(i + 4 < 17) vf[i + 4] = *(LAS v4u *)(kv + (i + 4) * 2048 + 1024);
        const f32x4 d = s[i] - m;
        v2u p;
        p.x = pk2(__builtin_amdgcn_exp2f(d[0]), __builtin_amdgcn_exp2f(d[1]));
        p.y = pk2(__builtin_amdgcn_exp2f(d[2]), __builtin_amdgcn_exp2f(d[3]));
        o0 = mfma16(v2u{vf[i].x, vf[i].y}, p, o0);
        o1 = mfma16(v2u{vf[i].z, vf[i].w}, p, o1);
        if(i < 8 && i * 8 + w < W1_EARLY) dma1k(rws, lane * 16, WT(ID_W1 + slot * 98 + i * 8 + w), ldsb + L_X + (i * 8 + w) * 1024);
        if(layer == 0 && i >= 8 && i < 16) W2LOAD(i - 8);
        if(i & 1) SB();
      }
      const float inv = __builtin_amdgcn_rcpf(__shfl(o1[0], 48 + li));
      o0 *= inv, o1 *= inv;
      const v4u of = {pk2(o0[0], o0[1]), pk2(o0[2], o0[3]), pk2(o1[0], o1[1]), pk2(o1[2], o1[3])};
      st16(of, rws, lane * 16, PB(t, layer) + FO(F_O + w * 16 + slot));
      STAMP(9 + layer * 30);
      DRAIN();
      BARRIER();
      STAMP(10 + layer * 30);
      if(w == 7 && lane == 0) set_word(ws, FLAG(t, layer, FL_O, slot), tag);
    }
    if(SLOT_OF(b0) < 14) {
      IDS();
      if(w == 7) wait_flags(ws, FLAG(t, layer, FL_O, li), tag);
      const rsrc_t rx = layer == 0 ? __builtin_amdgcn_make_buffer_rsrc((void *)x, 0, S * D * 2, 0x00020000) : rws;
      const v4u xr = ld16(rx, layer == 0 ? (16 * tt + li) * (D * 2) + g * 16 : lane * 16,
          layer == 0 ? slot * 64 : PB(t, 2) + FO(F_X2 + w * 16 + slot));
      BARRIER();
      STAMP(12 + layer * 30);
      v4u of[16];
      const int ob = opq(PB(t, layer) + FO(F_O + w * 16));
#pragma unroll
      for(int h = 0; h < 8; ++h) of[h] = ld16(rws, lane * 16 + (h & 3) * 1024, ob + X4(h));
      SB();
      v4u wf[32];
#pragma unroll
      for(int i = 0; i < 8; ++i) wf[i] = *(LAS v4u *)(L + L_WO + i * 1024 + lane * 16);
      SB();
      f32x4 a0 = {0.f, 0.f, 0.f, 0.f}, a1 = {0.f, 0.f, 0.f, 0.f};
#pragma unroll
      for(int h = 0; h < 16; ++h) {
        if(h < 8) of[h + 8] = ld16(rws, lane * 16 + (h & 3) * 1024, ob + X4(h + 8));
        if(h < 12)
          wf[2 * h + 8] = *(LAS v4u *)(L + L_WO + (2 * h + 8) * 1024 + lane * 16),
                     wf[2 * h + 9] = *(LAS v4u *)(L + L_WO + (2 * h + 9) * 1024 + lane * 16);
        a0 = mfma32(wf[2 * h], of[h], a0), a1 = mfma32(wf[2 * h + 1], of[h], a1);
        SB();
      }
      const v4u y = {pk2(a0[0] + bflo(xr.x), a0[1] + bfhi(xr.x)), pk2(a0[2] + bflo(xr.y), a0[3] + bfhi(xr.y)),
          pk2(a1[0] + bflo(xr.z), a1[1] + bfhi(xr.z)), pk2(a1[2] + bflo(xr.w), a1[3] + bfhi(xr.w))};
      st16(y, rws, lane * 16, PB(t, layer) + FO(F_X1 + w * 16 + slot));
      st8(piece_stats(y), rws, lane < 16 ? lane * 8 : OOB, PB(t, 2) + FO(F_ST + (layer * 8 + w) * 2) + slot * 128);
      STAMP(13 + layer * 30);
      DRAIN();
      BARRIER();
      if(w == 7 && lane == 0) set_word(ws, FLAG(t, layer, FL_X1, slot), tag);
    }
    STAMP(14 + layer * 30);
    {
      IDS();
      const int xb = opq(PB(t, layer) + FO(F_X1 + w * 16));
      if(w == 7) wait_flags(ws, FLAG(t, layer, FL_X1, (li < 14 ? li : 0)), tag);
      if(layer == 0 && ti < 112) {
        const int nt = ti >> 4, i = ti & 15;
        float sum = 0.f;
#pragma unroll
        for(int ks = 0; ks < 14; ++ks)
          sum += __uint_as_float(__builtin_amdgcn_raw_buffer_load_b32(rws, OFF_CSP + (ID_W1 + slot * 98 + ks * 7) * 64 + ti * 4, 0, AUX_SC1));
        *(LAS float *)(L + LC_CS1 + ti * 4) = sum;
        const int col = nt < 6 ? 112 * slot + 32 * (nt >> 1) + 8 * (i >> 2) + (i & 3) + 4 * (nt & 1) : 112 * slot + 96 + i;
        *(LAS float *)(L + LC_B1 + ti * 4) = bflo(b1[col]);
      }
      BARRIER();
      STAMP(15 + layer * 30);
      v4u xf[14];
      v2u sp[4];
      ROW_STATS(layer);
#pragma unroll
      for(int ks = 0; ks < XRING; ++ks) XLOAD(ks);
      SB();
      WT_DMA(ID_W1 + slot * 98, W1_EARLY, 98, w, 8, L_X);
      f32x4 acc[7];
#pragma unroll
      for(int n = 0; n < 7; ++n) acc[n] = f32x4{0.f, 0.f, 0.f, 0.f};
      float rs, nmr;
      const int wb = opq(ID_W2 + CSL(slot) * 224 + w * 28);
      SB();
#define STEP_B(ks) \
  if((ks) == 3) {   \
    SUM_STATS(); \
    const float mu = s1 * (1.f / D), var = fmaxf(s2 * (1.f / D) - mu * mu, 0.f), r1 = __builtin_amdgcn_rsqf(var + EPS); \
    rs = r1 * __builtin_amdgcn_rsqf(var * r1 * r1 + EPS), nmr = -mu * rs; \
    asm volatile("" : "+v"(rs), "+v"(nmr)); \
  } \
  STEP_X(ks); \
  if(layer == 0 && (ks) >= 6) { \
    _Pragma("unroll") for(int j = 8 + ((ks)-6) * 3; j < 8 + ((ks)-5) * 3 && j < 28; ++j) W2LOAD(j); \
  }
      GEMM_PART(7, 0, 7, 0, 9, 7, STEP_B);
      asm volatile("s_waitcnt vmcnt(%0)" ::"i"(14 - XRING) : "memory");
      BARRIER();
      GEMM_PART(7, 0, 7, 9, 14, 7, STEP_B);
      STAMP(16 + layer * 30);
      const int ho = PB(t, 0) + FO(F_H + w * 64 + (slot >> 1) * 8);
      f32x4 cc[7], bb[7];
#pragma unroll
      for(int n = 0; n < 7; ++n) cc[n] = *(LAS f32x4 *)(L + LC_CS1 + n * 64 + g * 16), bb[n] = *(LAS f32x4 *)(L + LC_B1 + n * 64 + g * 16);
      SB();
      unsigned hp[14];
#pragma unroll
      for(int n = 0; n < 7; ++n) {
        const f32x4 v = acc[n] * rs + (cc[n] * nmr + bb[n]);
        const f32x4 a = v * (v * v * -0.1029432396f + -2.3022081986f);
        const f32x4 e = {__builtin_amdgcn_exp2f(a[0]), __builtin_amdgcn_exp2f(a[1]), __builtin_amdgcn_exp2f(a[2]), __builtin_amdgcn_exp2f(a[3])};
        const f32x4 d = e + 1.f;
        const f32x4 r = {__builtin_amdgcn_rcpf(d[0]), __builtin_amdgcn_rcpf(d[1]), __builtin_amdgcn_rcpf(d[2]), __builtin_amdgcn_rcpf(d[3])};
        const f32x4 hv = v * r;
        hp[2 * n] = pk2(hv[0], hv[1]), hp[2 * n + 1] = pk2(hv[2], hv[3]);
        if(n & 1)
          st16(v4u{hp[2 * n - 2], hp[2 * n - 1], hp[2 * n], hp[2 * n + 1]}, rws, lane * 16,
              ho + (n < 4 ? 2 * (slot & 1) + (n >> 1) : 4 + (slot & 1)) * 1024);
        if(n == 6) st8(v2u{hp[12], hp[13]}, rws, lane * 16, ho + 6144 + (slot & 1) * 8);
      }
      STAMP(17 + layer * 30);
      DRAIN();
      BARRIER();
      STAMP(18 + layer * 30);
      if(w == 7) {
        if(lane == 0) set_word(ws, FLAG(t, layer, FL_H, slot), tag);
        if(slot < 14) wait_flags(ws, FLAG(t, layer, FL_H, li), tag);
      } else if(layer == 0 && slot < 14) {
        WT_DMA(ID_WQ + slot * 84, 0, WQ_HOP, w, 7, L_X);
      }
    }
    if(SLOT_OF(b0) < 14) {
      IDS();
      const int csl = CSL(slot), chalf = slot >= 7;
      BARRIER();
      STAMP(19 + layer * 30);
      const int ho = opq(PB(t, 0) + FO(F_H + 4 * chalf * 64 + w * 8));
      v4u hf[8];
#pragma unroll
      for(int i = 0; i < 8; ++i) hf[i] = ld16(rws, lane * 16 + (i >> 2) * 1024, ho + (i & 3) * FO(64));
      const int ow = 4 * chalf + (w >> 1), oks = 2 * csl + (w & 1);
      v4u xr, bb;
      SB();
      f32x4 acc[4][4];
#pragma unroll
      for(int q = 0; q < 4; ++q)
#pragma unroll
        for(int n = 0; n < 4; ++n) acc[q][n] = f32x4{0.f, 0.f, 0.f, 0.f};
#pragma unroll
      for(int k = 0; k < 7; ++k) {
#pragma unroll
        for(int q = 0; q < 4; ++q) {
          const v4u cur = hf[(k & 1) * 4 + q];
          if(k + 2 < 7) hf[(k & 1) * 4 + q] = ld16(rws, lane * 16 + (k & 3) * 1024, ho + q * FO(64) + 2048 + (k >> 2) * 4096);
          if(k == 5 && q == 1) xr = ld16(rws, lane * 16, opq(PB(t, layer) + FO(F_X1 + ow * 16 + oks)));
          if(k == 5 && q == 2)
            bb = __builtin_amdgcn_raw_buffer_load_b128(__builtin_amdgcn_make_buffer_rsrc((void *)b2, 0, D * 2, 0x00020000), g * 16, opq(oks * 64), 0);
#pragma unroll
          for(int n = 0; n < 4; ++n) acc[q][n] = mfma32(w2f[k * 4 + n], cur, acc[q][n]);
          SB();
        }
      }
      STAMP(20 + layer * 30);
      if(layer == 0) WT_DMA(ID_WQ + slot * 84, WQ_HOP, 84, w, 8, L_X);
      f32x4 a0, a1;
#pragma unroll
      for(int f = 0; f < 8; ++f) {
        const f32x4 p0 = acc[f >> 1][2 * (f & 1)], p1 = acc[f >> 1][2 * (f & 1) + 1];
        if(f == w) a0 = p0, a1 = p1;
        else
          *(LAS v4u *)(L + L_RED + (f * 7 + (w < f ? w : w - 1)) * 1024 + lane * 16) =
              v4u{pk2(p0[0], p0[1]), pk2(p0[2], p0[3]), pk2(p1[0], p1[1]), pk2(p1[2], p1[3])};
      }
      BARRIER();
      STAMP(21 + layer * 30);
      {
        const unsigned one = g == (li >> 2) ? ((li & 1) ? 0x3F800000u : 0x00003F80u) : 0u;
        const v4u e0 = {(li & 2) ? 0u : one, (li & 2) ? one : 0u, 0u, 0u}, e1 = {0u, 0u, (li & 2) ? 0u : one, (li & 2) ? one : 0u};
        v4u p[7];
#pragma unroll
        for(int s = 0; s < 7; ++s) p[s] = *(LAS v4u *)(L + L_RED + (w * 7 + s) * 1024 + lane * 16);
        a0 = mfma32(e0, xr, a0), a1 = mfma32(e1, xr, a1);
        a0 = mfma32(e0, bb, a0), a1 = mfma32(e1, bb, a1);
#pragma unroll
        for(int s = 0; s < 7; ++s) a0 = mfma32(e0, p[s], a0), a1 = mfma32(e1, p[s], a1);
      }
      const v4u y = {pk2(a0[0], a0[1]), pk2(a0[2], a0[3]), pk2(a1[0], a1[1]), pk2(a1[2], a1[3])};
      if(layer == 0) {
        st16(y, rws, lane * 16, PB(t, 2) + FO(F_X2 + ow * 16 + oks));
        st8(piece_stats(y), rws, lane < 16 ? lane * 8 : OOB, PB(t, 2) + FO(F_ST + (16 + ow) * 2) + oks * 128);
        STAMP(22 + layer * 30);
      } else {
        *(GAS v4u *)(out + (16 * (8 * t + ow) + li) * D + 32 * oks + 8 * g) = y;
      }
    }
    STAMP(23 + layer * 30);
    if(layer == 0) {
      IDS();
      if(slot >= 14) WT_DMA(ID_WQ + slot * 84, 0, 84, w, 8, L_X);
      DRAIN();
      BARRIER();
      if(w == 7) {
        if(lane == 0 && slot < 14) set_word(ws, FLAG(t, 0, FL_X2, slot), tag);
        wait_flags(ws, FLAG(t, 0, FL_X2, (li < 14 ? li : 0)), tag);
      }
      BARRIER();
    }
  };
  run_layer(IntC<0>{});
  run_layer(IntC<1>{});
  STAMP(61);
}
}

extern "C" void kernel_launch(void *const *d_in, const int *in_sizes, int n_in, void *d_out, int out_size, void *d_ws, size_t ws_size,
    hipStream_t stream) {
  const size_t pad = (size_t)(-(intptr_t)d_ws) & (size_t)0x1FFFFF;
  fused_kernel<<<NWG, NTHR, 0, stream>>>((const u16 *)d_in[0], (const u16 *)d_in[1], (const u16 *)d_in[2], (const u16 *)d_in[4],
      (const u16 *)d_in[5], (const u16 *)d_in[6], (const u16 *)d_in[7], (u16 *)d_out, (unsigned char *)d_ws + pad);
}
```

```cpp
#include <hip/hip_runtime.h>
#include <stdint.h>

#define GAS __attribute__((address_space(1)))
#define LAS __attribute__((address_space(3)))
#define DI __device__ __forceinline__
typedef unsigned v4u __attribute__((ext_vector_type(4)));
typedef unsigned v2u __attribute__((ext_vector_type(2)));
typedef float f32x4 __attribute__((ext_vector_type(4)));
typedef float f32x2 __attribute__((ext_vector_type(2)));
typedef __bf16 bf16x8 __attribute__((ext_vector_type(8)));
typedef __bf16 bf16x2 __attribute__((ext_vector_type(2)));
typedef short s16x4 __attribute__((ext_vector_type(4)));
typedef unsigned long long u64;
typedef unsigned short u16;
typedef __amdgpu_buffer_rsrc_t rsrc_t;

#define TILE_OF(b) ((((b)&7) >> 1) * 4 + ((b) >> 6))
#define SLOT_OF(b) (((b)&1) * 8 + (((b) >> 3) & 7))
#define CS14(s) ((s) >= 14 ? 0 : (s))
#define CSL(s) ((s) >= 14 ? 0 : (s) >= 7 ? (s)-7 : (s))

#define CODEPF 1
#define TLBPF 0
namespace {
template <int V> struct IntC {
  static constexpr int value = V;
};
constexpr int S = 2048, D = 448, F = 1792;
constexpr int NWG = 256, NTHR = 512;
constexpr float EPS = 1e-5f;
constexpr int AUX_SC1 = 16;
constexpr unsigned SPIN_CAP = 1u << 16;
constexpr int OOB = 0x7F000000;
constexpr int XRING = 8;
constexpr int WDEP = 7;
constexpr u64 VOTE = 1ull << 63;

constexpr int FS = 128;
constexpr int OFF_EPOCH = 0, OFF_PREPG = 4096, OFF_PREPF = 8192, OFF_SEL = 49152;
constexpr int OFF_CSP = 1 << 20;
constexpr int OFF_WT = 2 << 20;
constexpr int ID_WO = 0, ID_W1 = 448, ID_W2 = 2016, ID_WQ = 3584, ID_END = 4928;
constexpr int OFF_PROBE = 12 << 20, OFF_STAMP = 14 << 20;
constexpr int PG0 = 8, WS_BYTES = (PG0 + 48) << 21;
constexpr int F_KV = 0, F_O = 256, F_X1 = 384, F_H = 512, F_X2 = 0, F_ST = 128, F_FL = 192;
constexpr int FL_KV = 0, FL_O = 1, FL_X1 = 2, FL_H = 3, FL_X2 = 4;
#define FO(f) ((((f) >> 3) << 14) + (((f)&7) << 10))
#define PB(t_, p_) (((PG0 + 3 * (t_) + (p_)) << 21) + (((side ^ (t_) ^ (p_) ^ ((t_) >> 3)) & 1) << 13))
#define FLAG(t_, layer_, ph_, i_) (PB(t_, 2) + FO(F_FL + ((layer_)*5 + (ph_)) * 2) + (i_)*FS)
#define WT(f) (OFF_WT + (f)*1024)
static_assert(OFF_CSP + ID_END * 64 <= OFF_WT && OFF_WT + ID_END * 1024 <= OFF_PROBE && (PG0 & 1) == 0, "map");

constexpr int L_X = 0;
constexpr int L_KV = 100352 - 32768;
constexpr int L_WO = 100352 + 16384;
constexpr int L_RED = 86016;
constexpr int L_C = L_WO + 32768;
constexpr int LC_CSQ = L_C, LC_CS1 = L_C + 512, LC_B1 = L_C + 1024;
constexpr int LS_Q = L_C + 1536, LS_HOLD = LS_Q + 4, LS_KV = LS_Q + 8, LS_SIDE = LS_Q + 32;
constexpr int LDS_BYTES = L_C + 2048;
constexpr int W1_EARLY = 63;
constexpr int WQ_HOP = 28;
static_assert(W1_EARLY * 1024 <= L_KV && L_RED + 56 * 1024 <= L_C, "LDS map");

DI f32x4 mfma32(v4u a, v4u b, f32x4 c) {
  return __builtin_amdgcn_mfma_f32_16x16x32_bf16(__builtin_bit_cast(bf16x8, a), __builtin_bit_cast(bf16x8, b), c, 0, 0, 0);
}
DI f32x4 mfma16(v2u a, v2u b, f32x4 c) {
  return __builtin_amdgcn_mfma_f32_16x16x16bf16_1k(__builtin_bit_cast(s16x4, a), __builtin_bit_cast(s16x4, b), c, 0, 0, 0);
}
DI unsigned pk2(float a, float b) {
  f32x2 v = {a, b};
  return __builtin_bit_cast(unsigned, __builtin_convertvector(v, bf16x2));
}
DI float bflo(unsigned u) { return __uint_as_float(u << 16); }
DI float bfhi(unsigned u) { return __uint_as_float(u & 0xFFFF0000u); }
DI float dot2(unsigned a, unsigned b, float c) {
  return __builtin_amdgcn_fdot2_f32_bf16(__builtin_bit_cast(bf16x2, a), __builtin_bit_cast(bf16x2, b), c, false);
}
DI float gsum(float x) {
  auto r = __builtin_amdgcn_permlane32_swap(__float_as_uint(x), __float_as_uint(x), false, false);
  x = __uint_as_float(r[0]) + __uint_as_float(r[1]);
  auto q = __builtin_amdgcn_permlane16_swap(__float_as_uint(x), __float_as_uint(x), false, false);
  return __uint_as_float(q[0]) + __uint_as_float(q[1]);
}
DI float gmax(float x) {
  auto r = __builtin_amdgcn_permlane32_swap(__float_as_uint(x), __float_as_uint(x), false, false);
  x = fmaxf(fmaxf(x, __uint_as_float(r[0])), __uint_as_float(r[1]));
  auto q = __builtin_amdgcn_permlane16_swap(__float_as_uint(x), __float_as_uint(x), false, false);
  return fmaxf(fmaxf(x, __uint_as_float(q[0])), __uint_as_float(q[1]));
}
DI v4u ld16(rsrc_t rs, int voff, int soff) { return __builtin_amdgcn_raw_buffer_load_b128(rs, voff, soff, AUX_SC1); }
DI v2u ld8(rsrc_t rs, int voff, int soff) { return __builtin_amdgcn_raw_buffer_load_b64(rs, voff, soff, AUX_SC1); }
DI void st16(v4u v, rsrc_t rs, int voff, int soff) {
  __builtin_amdgcn_raw_buffer_store_b128(v, rs, voff, soff, AUX_SC1);
  asm volatile("s_nop 1" ::"v"(v) : "memory");
}
DI void st8(v2u v, rsrc_t rs, int voff, int soff) { __builtin_amdgcn_raw_buffer_store_b64(v, rs, voff, soff, AUX_SC1); }
DI int opq(int v) {
  asm volatile("" : "+s"(v));
  return v;
}
DI int opqv(int v) {
  asm volatile("" : "+v"(v));
  return v;
}
DI v2u trread(LAS unsigned char *p) { return __builtin_bit_cast(v2u, __builtin_amdgcn_ds_read_tr16_b64_v4i16((LAS s16x4 *)p)); }
#define DRAIN() asm volatile("s_waitcnt vmcnt(0)" ::: "memory")
#define BARRIER() asm volatile("s_waitcnt lgkmcnt(0)\n\ts_barrier" ::: "memory")
#define SB() __builtin_amdgcn_sched_barrier(0)
#define dma1k(rs, voff, soff, ldsaddr) \
  asm volatile("s_mov_b32 m0, %0\n\ts_nop 0\n\tbuffer_load_dwordx4 %1, %2, %3 offen sc1 lds" ::"s"((unsigned)(ldsaddr)), "v"((int)(voff)), \
               "s"(rs), "s"((int)(soff)) \
               : "memory")
#define dma1k_input(rs, voff, soff, ldsaddr) \
  asm volatile("s_mov_b32 m0, %0\n\ts_nop 0\n\tbuffer_load_dwordx4 %1, %2, %3 offen lds" ::"s"((unsigned)(ldsaddr)), "v"((int)(voff)), \
               "s"(rs), "s"((int)(soff)) \
               : "memory")
DI void wait_flags(const unsigned char *ws, int off, u64 tag) {
  const GAS u64 *p = (const GAS u64 *)(ws + off);
  for(unsigned spin = 0; spin < SPIN_CAP; ++spin) {
    const u64 v = __hip_atomic_load(p, __ATOMIC_RELAXED, __HIP_MEMORY_SCOPE_AGENT);
    if(__all(v == tag)) break;
    __builtin_amdgcn_s_sleep(1);
  }
  __builtin_amdgcn_fence(__ATOMIC_ACQUIRE, "wavefront");
}
DI void set_word(unsigned char *ws, int off, u64 v) {
  __hip_atomic_store((GAS u64 *)(ws + off), v, __ATOMIC_RELAXED, __HIP_MEMORY_SCOPE_AGENT);
}
DI unsigned lds_count(LAS unsigned char *p, unsigned add, int lane) {
  unsigned old = 0;
  if(lane == 0) old = __hip_atomic_fetch_add((LAS unsigned *)p, add, __ATOMIC_RELAXED, __HIP_MEMORY_SCOPE_WORKGROUP);
  return __builtin_amdgcn_readfirstlane(old);
}
DI v2u piece_stats(v4u y) {
  float s1 = 0.f, s2 = 0.f;
#pragma unroll
  for(int j = 0; j < 4; ++j) s1 = dot2(y[j], 0x3F803F80u, s1), s2 = dot2(y[j], y[j], s2);
  return v2u{__float_as_uint(gsum(s1)), __float_as_uint(gsum(s2))};
}

#define STAMP(i)

__global__ void __launch_bounds__(NTHR, 2)
    fused_kernel(const u16 *__restrict__ x, const u16 *__restrict__ wqkv, const u16 *__restrict__ wo, const u16 *__restrict__ w1,
        const u16 *__restrict__ b1, const u16 *__restrict__ w2, const u16 *__restrict__ b2, u16 *__restrict__ out, unsigned char *ws) {
  __shared__ __attribute__((aligned(16))) unsigned char smem[LDS_BYTES];
  LAS unsigned char *L = (LAS unsigned char *)smem;
  const unsigned ldsb = (unsigned)(size_t)L;
  const int tid = threadIdx.x, w0 = __builtin_amdgcn_readfirstlane(tid >> 6), b0 = blockIdx.x;
#define IDS() \
  const int ti = opqv(tid), lane = ti & 63, w = opq(w0), b = opq(b0), t = TILE_OF(b), slot = SLOT_OF(b); \
  const int tt = 8 * t + w, g = lane >> 4, li = lane & 15; \
  (void)g, (void)li, (void)tt, (void)slot
#define WT_DMA(id0, from, to, w_, nw_, ldsoff) \
  for(int i_ = (from) + (w_); i_ < (to); i_ += (nw_)) dma1k(rws, lane * 16, WT((id0) + i_), ldsb + (ldsoff) + i_ * 1024)
#define GEMM_PART(NT, N0, NN, K0, K1, SWAPN, STEP) \
  { \
    constexpr int CNT = ((K1) - (K0)) * (NN); \
    v4u wr[WDEP]; \
    _Pragma("unroll") for(int i = 0; i < WDEP; ++i) wr[i] = *(LAS v4u *)(L + L_X + (((K0) + i / (NN)) * (NT) + (N0) + i % (NN)) * 1024 + lane * 16); \
    SB(); \
    _Pragma("unroll") for(int i = 0; i < CNT; ++i) { \
      const int ks = (K0) + i / (NN), n = (N0) + i % (NN); \
      if(i % (NN) == 0) { \
        STEP(ks); \
      } \
      const v4u cur = wr[i % WDEP]; \
      if(i + WDEP < CNT) \
        wr[i % WDEP] = *(LAS v4u *)(L + L_X + (((K0) + (i + WDEP) / (NN)) * (NT) + (N0) + (i + WDEP) % (NN)) * 1024 + lane * 16); \
      acc[n] = n < (SWAPN) ? mfma32(cur, xf[ks], acc[n]) : mfma32(xf[ks], cur, acc[n]); \
      SB(); \
    } \
  }
#define X4(k) ((((k) >> 3) << 14) + ((((k) >> 2) & 1) << 12))
#define XLOAD(k) xf[k] = ld16(rws, lane * 16 + ((k)&3) * 1024, xb + X4(k))
#define STEP_NONE(ks)
#define STEP_X(ks) \
  if((ks) + XRING < 14) XLOAD((ks) + XRING)
#define ROW_STATS(which) \
  _Pragma("unroll") for(int i = 0; i < 4; ++i) \
      sp[i] = ld8(rws, (i == 3 && g > 1) ? OOB : li * 8 + g * 128 + i * 512, PB(t, 2) + FO(F_ST + ((which)*8 + w) * 2))
#define SUM_STATS() \
  const float s1 = gsum((__uint_as_float(sp[0].x) + __uint_as_float(sp[1].x)) + (__uint_as_float(sp[2].x) + __uint_as_float(sp[3].x))); \
  const float s2 = gsum((__uint_as_float(sp[0].y) + __uint_as_float(sp[1].y)) + (__uint_as_float(sp[2].y) + __uint_as_float(sp[3].y)))
#define W2LOAD(j) w2f[j] = ld16(rws, lane * 16 + ((j)&3) * 1024, WT(wb + ((j) & ~3)))
  const rsrc_t rws = __builtin_amdgcn_make_buffer_rsrc((void *)ws, 0, WS_BYTES, 0x00020000);
  STAMP(0);
  u64 tag = 0;
  int side = 0;
  {
    u64 pc;
    int len;
    asm volatile("s_getpc_b64 %0\n.Lfk_here:\n\ts_mov_b32 %1, .Lfk_end-.Lfk_here" : "=s"(pc), "=s"(len));
    if(CODEPF && w0 == 7) {
      const rsrc_t rc = __builtin_amdgcn_make_buffer_rsrc((void *)pc, 0, len, 0x00020000);
      asm volatile("s_nop 4" ::: "memory");
      dma1k_input(rc, (tid & 63) * 16, (b0 >> 3) * 2048, ldsb + L_WO + 30720);
      dma1k_input(rc, (tid & 63) * 16, (b0 >> 3) * 2048 + 1024, ldsb + L_WO + 31744);
    }
    if(TLBPF && w0 == 6)
      asm volatile("s_mov_b32 m0, %0\n\ts_nop 0\n\tbuffer_load_dword %1, %2, 0 offen sc1 lds" ::"s"(ldsb + L_WO + 28672),
                   "v"((tid & 63) < 9 ? (max(PG0 + 3 * (TILE_OF(b0) - 2) + (tid & 63), 0) << 21) + 0x1FD000 + (b0 & 31) * 128 : OOB), "s"(rws)
                   : "memory");
  }
  v4u w2f[28];

  auto run_layer = [&](auto LC) __attribute__((always_inline)) {
    constexpr int layer = decltype(LC)::value;
    v4u qf;
    {
      IDS();
      v4u xf[14];
      float mu, rs, mur[4], rsr[4];
      f32x4 acc[6];
#pragma unroll
      for(int n = 0; n < 6; ++n) acc[n] = f32x4{0.f, 0.f, 0.f, 0.f};
#define A_FINISH_STATS() \
  mu = s1 * (1.f / D), rs = __builtin_amdgcn_rsqf(fmaxf(s2 * (1.f / D) - mu * mu, 0.f) + EPS); \
  _Pragma("unroll") for(int r = 0; r < 4; ++r) mur[r] = __shfl(mu, 4 * g + r), rsr[r] = __shfl(rs, 4 * g + r); \
  asm volatile("" : "+v"(mu), "+v"(rs), "+v"(mur[0]), "+v"(mur[1]), "+v"(mur[2]), "+v"(mur[3]), "+v"(rsr[0]), "+v"(rsr[1]), "+v"(rsr[2]), "+v"(rsr[3]))
      if(layer == 0) {
        int dst[2], dst1[2], tro[2], tre[2];
        unsigned char *pp = ws + OFF_PROBE + (b & 31) * 16384 + w * 512;
        if(w < 3 && b >= 224) {
          asm volatile("global_store_dword %0, %1, off sc1" ::"v"(pp + (lane & 1) * 8192 + ((lane >> 1) & 1) * 128), "v"(0u) : "memory");
        } else if(w < 3) {
          const int r = lane >> 2, c = lane & 3, head = b / 14, ks = b - 14 * head;
          const rsrc_t rq = __builtin_amdgcn_make_buffer_rsrc((void *)wqkv, 0, D * 3 * D * 2, 0x00020000);
          const int o0 = (32 * ks + r) * 2688 + w * 896 + 56 * head + c * 16;
          dma1k_input(rq, o0, 0, ldsb + L_WO + w * 2048);
          dma1k_input(rq, o0, 16 * 2688, ldsb + L_WO + w * 2048 + 1024);
          dst[0] = ID_WQ + head * 84 + ks * 6 + w * 2, dst1[0] = dst[0] + 1, tro[0] = 16, tre[0] = 8;
        }
        v2u ep;
        asm volatile("buffer_load_dwordx2 %0, off, %1, 0 sc1" : "=v"(ep) : "s"(rws) : "memory");
        if(ti < 8) *(LAS unsigned *)(L + LS_Q + 4 * ti) = 0u;
        const rsrc_t rx = __builtin_amdgcn_make_buffer_rsrc((void *)x, 0, S * D * 2, 0x00020000);
        const int xvo = (16 * tt + (lane >> 3)) * (D * 2) + (((lane & 7) ^ (lane >> 3)) * 16);
#define X0_DMA() \
  _Pragma("unroll") for(int i = 0; i < 14; ++i) dma1k_input(rx, xvo, (i & 1) * 8 * (D * 2) + (i >> 1) * 128, ldsb + w * 14336 + i * 1024)
#define TAKE_TAG() \
  asm volatile("s_waitcnt vmcnt(0)" : "+v"(ep)::"memory"); \
  tag = (((u64)(unsigned)__builtin_amdgcn_readfirstlane(ep.y) << 32) | (unsigned)__builtin_amdgcn_readfirstlane(ep.x)) + 1ull
#define PREP_OUT(rd, QPAD) \
  { \
    LAS unsigned char *p = L + L_WO + ((rd)*8 + w) * 2048 + (8 * g + (li >> 2)) * 64 + (li & 3) * tro[rd]; \
    const v2u a0 = trread(p), a1 = trread(p + 256), c0 = trread(p + tre[rd]), c1 = trread(p + tre[rd] + 256); \
    const v4u f0 = {a0.x, a0.y, a1.x, a1.y}; \
    v4u f1 = {c0.x, c0.y, c1.x, c1.y}; \
    if(QPAD && li >= 12) f1 = v4u{0u, 0u, 0u, 0u}; \
    const bool ok = dst[rd] < ID_END; \
    st16(f0, rws, ok ? lane * 16 : OOB, WT(dst[rd])); \
    st16(f1, rws, ok ? lane * 16 : OOB, WT(dst1[rd])); \
    float ca = 0.f, cb = 0.f; \
    _Pragma("unroll") for(int j = 0; j < 4; ++j) ca = dot2(f0[j], 0x3F803F80u, ca), cb = dot2(f1[j], 0x3F803F80u, cb); \
    ca = gsum(ca), cb = gsum(cb); \
    __builtin_amdgcn_raw_buffer_store_b32( \
        __float_as_uint(g ? cb : ca), rws, (ok && g < 2) ? OFF_CSP + (g ? dst1[rd] : dst[rd]) * 64 + li * 4 : OOB, 0, AUX_SC1); \
  }
        BARRIER();
        if(w < 3) {
          unsigned vote = 1u;
          TAKE_TAG();
          STAMP(25);
          if(b >= 224) {
            unsigned v;
            unsigned best[2] = {~0u, ~0u};
#pragma unroll
            for(int i = 0; i < 4; ++i) {
              u64 t0, t1;
              asm volatile(
                  "s_memtime %0\n\ts_waitcnt lgkmcnt(0)\n\tglobal_load_dword %2, %3, off sc1\n\ts_waitcnt vmcnt(0)\n\ts_memtime %1\n\ts_waitcnt lgkmcnt(0)"
                  : "=&s"(t0), "=&s"(t1), "=&v"(v)
                  : "v"(pp + ((i ^ w ^ (b >> 3)) & 1) * 8192 + (i >> 1) * 128)
                  : "memory");
              const unsigned dt = (unsigned)(t1 - t0);
              if((i ^ w ^ (b >> 3)) & 1) best[1] = min(best[1], dt);
              else best[0] = min(best[0], dt);
            }
            if((best[1] < best[0]) != (bool)((b >> 2) & 1)) vote = 0x101u;
          } else {
            PREP_OUT(0, true);
          }
          lds_count(L + LS_HOLD, 1u, lane);
          DRAIN();
          STAMP(26);
          const unsigned cnt = lds_count(L + LS_Q, vote, lane) + vote;
          if((cnt & 0xFFu) == 3u && lane == 0) set_word(ws, OFF_PREPF + b * FS, cnt >= 0x200u ? tag ^ VOTE : tag);
          X0_DMA();
          DRAIN();
        } else {
#pragma unroll
          for(int rd = 0; rd < 2; ++rd) {
            const int id = rd == 0 ? b * 5 + (w - 3) : w < 5 ? 1280 + b * 2 + (w - 3) : 1792, r = lane >> 2, c = lane & 3;
            const u16 *src;
            int bytes, o0, o1;
            tro[rd] = 16, tre[rd] = 8;
            if(id < 224) {
              const int ks = id / 14, sl = id - ks * 14;
              src = wo, bytes = D * D * 2;
              o0 = (28 * ks + r) * 896 + 64 * sl + c * 16, o1 = r < 12 ? o0 + 16 * 896 : OOB;
              dst[rd] = ID_WO + sl * 32 + ks * 2, dst1[rd] = dst[rd] + 1;
            } else if(id < 224 + 672) {
              const int i2 = id - 224, ks = i2 / 48, rem = i2 - ks * 48, sl = rem / 3, p = rem - sl * 3;
              src = w1, bytes = D * F * 2;
              o0 = (32 * ks + r) * 3584 + 224 * sl + 64 * p + c * 16, o1 = o0 + 16 * 3584;
              dst[rd] = ID_W1 + sl * 98 + ks * 7 + 2 * p, dst1[rd] = dst[rd] + 1;
            } else if(id < 896 + 112) {
              const int i2 = id - 896, ks = i2 >> 3, sl = 2 * (i2 & 7);
              src = w1, bytes = D * F * 2;
              o0 = (32 * ks + r) * 3584 + 224 * (sl + (c >> 1)) + 192 + (c & 1) * 16, o1 = o0 + 16 * 3584;
              dst[rd] = ID_W1 + sl * 98 + ks * 7 + 6, dst1[rd] = dst[rd] + 98, tro[rd] = 8, tre[rd] = 32;
            } else {
              const int i2 = id - 1008, hks = i2 / 14, cb = i2 - hks * 14, wv = hks / 7, kk = hks - 7 * wv;
              src = w2, bytes = F * D * 2;
              const int row = 224 * wv +
                  (kk < 4 ? 112 * (kk >> 1) + 32 * (kk & 1) + r : kk < 6 ? 112 * (kk - 4) + 64 + r : 96 + 112 * ((r >> 2) & 1) + 4 * (r >> 3) + (r & 3));
              o0 = row * 896 + 64 * cb + c * 16, o1 = o0 + (kk < 6 ? 16 : 8) * 896;
              dst[rd] = ID_W2 + (cb >> 1) * 224 + hks * 4 + (cb & 1) * 2, dst1[rd] = dst[rd] + 1;
              if(id >= 1792) o0 = o1 = OOB, dst[rd] = dst1[rd] = ID_END;
            }
            const rsrc_t rsw = __builtin_amdgcn_make_buffer_rsrc((void *)src, 0, bytes, 0x00020000);
            asm volatile("s_nop 4" ::: "memory");
            dma1k_input(rsw, o0, 0, ldsb + L_WO + (rd * 8 + w) * 2048);
            dma1k_input(rsw, o1, 0, ldsb + L_WO + (rd * 8 + w) * 2048 + 1024);
          }
          for(unsigned spin = 0; spin < (1u << 20); ++spin)
            if(__builtin_amdgcn_readfirstlane(__hip_atomic_load((LAS unsigned *)(L + LS_HOLD), __ATOMIC_RELAXED, __HIP_MEMORY_SCOPE_WORKGROUP)) >= 3)
              break;
          X0_DMA();
          STAMP(25);
          TAKE_TAG();
          STAMP(26);
          if(w == 7) wait_flags(ws, OFF_PREPF + (slot * 14 + (li < 14 ? li : 0)) * FS, tag);
        }
        STAMP(27);
#pragma unroll
        for(int ks = 0; ks < 14; ++ks)
          xf[ks] = *(LAS v4u *)(L + w * 14336 + ((ks >> 1) * 2 + (li >> 3)) * 1024 + (li & 7) * 128 + (((ks & 1) * 4 + g) ^ (li & 7)) * 16);
        BARRIER();
        STAMP(2);
        WT_DMA(ID_WQ + slot * 84, 0, 84, w, 8, L_X);
        {
          float s1 = 0.f, s2 = 0.f, s3 = 0.f, s4 = 0.f;
#pragma unroll
          for(int ks = 0; ks < 14; ++ks) {
            s1 = dot2(xf[ks][0], 0x3F803F80u, s1), s2 = dot2(xf[ks][0], xf[ks][0], s2);
            s3 = dot2(xf[ks][1], 0x3F803F80u, s3), s4 = dot2(xf[ks][1], xf[ks][1], s4);
            s1 = dot2(xf[ks][2], 0x3F803F80u, s1), s2 = dot2(xf[ks][2], xf[ks][2], s2);
            s3 = dot2(xf[ks][3], 0x3F803F80u, s3), s4 = dot2(xf[ks][3], xf[ks][3], s4);
          }
          s1 = gsum(s1 + s3), s2 = gsum(s2 + s4);
          A_FINISH_STATS();
        }
        unsigned cv[14];
        if(w >= 3) {
          PREP_OUT(0, false);
          PREP_OUT(1, false);
          asm volatile("s_waitcnt vmcnt(11)" ::: "memory");
        } else {
          asm volatile("s_waitcnt vmcnt(5)" ::: "memory");
#pragma unroll
          for(int ks = 0; ks < 14; ++ks)
            cv[ks] = __builtin_amdgcn_raw_buffer_load_b32(rws, ti < 96 ? OFF_CSP + (ID_WQ + slot * 84 + ks * 6) * 64 + ti * 4 : OOB, 0, AUX_SC1);
          SB();
        }
        BARRIER();
        STAMP(3);
        GEMM_PART(6, 2, 4, 0, 7, 4, STEP_NONE);
        if(ti < 96) {
          float csum = 0.f;
#pragma unroll
          for(int ks = 0; ks < 14; ++ks) csum += __uint_as_float(cv[ks]);
          *(LAS float *)(L + LC_CSQ + ti * 4) = csum;
        }
        if(w == 2) {
          u64 v = 0;
          for(unsigned spin = 0; spin < SPIN_CAP; ++spin) {
            v = __hip_atomic_load((const GAS u64 *)(ws + OFF_PREPF + (224 + (lane & 31)) * FS), __ATOMIC_RELAXED, __HIP_MEMORY_SCOPE_AGENT);
            if(__all((v | VOTE) == (tag | VOTE))) break;
          }
          const int ones = __popcll(__ballot(v != tag));
          if(lane == 0) *(LAS int *)(L + LS_SIDE) = ones > 32;
        }
        DRAIN();
        BARRIER();
        side = __builtin_amdgcn_readfirstlane(*(LAS int *)(L + LS_SIDE));
        if(ti == 0) set_word(ws, OFF_PREPG + b * 8, tag);
        GEMM_PART(6, 2, 4, 7, 14, 4, STEP_NONE);
      } else {
        STAMP(31);
        const int xb = opq(PB(t, 2) + FO(F_X2 + w * 16));
        v2u sp[4];
        ROW_STATS(2);
#pragma unroll
        for(int ks = 0; ks < XRING; ++ks) XLOAD(ks);
        SB();
        STAMP(33);
#define STEP_XA(ks) \
  if((ks) == 3) { \
    SUM_STATS(); \
    A_FINISH_STATS(); \
  } \
  STEP_X(ks)
        GEMM_PART(6, 2, 4, 0, 14, 4, STEP_XA);
      }
      STAMP(4 + layer * 30);
      const float qs = rs * (0.18898223650461362f * 1.4426950408889634f);
      f32x4 c[4];
#pragma unroll
      for(int n = 0; n < 4; ++n) c[n] = *(LAS f32x4 *)(L + LC_CSQ + n * 64 + g * 16);
      const float c4 = *(LAS float *)(L + LC_CSQ + 256 + li * 4), c5 = *(LAS float *)(L + LC_CSQ + 320 + li * 4);
      f32x4 a0 = (acc[2] - mu * c[2]) * rs, a1 = (acc[3] - mu * c[3]) * rs;
      const v4u kf = {pk2(a0[0], a0[1]), pk2(a0[2], a0[3]), g == 3 ? 0u : pk2(a1[0], a1[1]), g == 3 ? 0u : pk2(a1[2], a1[3])};
#pragma unroll
      for(int r = 0; r < 4; ++r) a0[r] = (acc[4][r] - mur[r] * c4) * rsr[r], a1[r] = (acc[5][r] - mur[r] * c5) * rsr[r];
      const unsigned vpad = li == 12 ? 0x3F803F80u : 0u;
      const v4u vf = {pk2(a0[0], a0[1]), pk2(a0[2], a0[3]), li >= 12 ? vpad : pk2(a1[0], a1[1]), li >= 12 ? vpad : pk2(a1[2], a1[3])};
      const int kvo = PB(t, layer) + FO(F_KV + (slot * 8 + w) * 2);
      st16(kf, rws, lane * 16, kvo);
      st16(vf, rws, lane * 16 + 1024, kvo);
      *(LAS v4u *)(L + L_KV + (16 + w) * 2048 + lane * 16) = kf;
      *(LAS v4u *)(L + L_KV + (16 + w) * 2048 + 1024 + lane * 16) = vf;
      STAMP(5 + layer * 30);
#define STEP_KVF(ks) \
  if((ks) == 9) { \
    DRAIN(); \
    if(lds_count(L + LS_KV, 1u, lane) == 8u * (layer + 1) - 1u && lane == 0) set_word(ws, FLAG(t, layer, FL_KV, slot), tag); \
    SB(); \
  }
      GEMM_PART(6, 0, 2, 0, 14, 4, STEP_KVF);
      a0 = (acc[0] - mu * c[0]) * qs, a1 = (acc[1] - mu * c[1]) * qs;
      qf = v4u{pk2(a0[0], a0[1]), pk2(a0[2], a0[3]), g == 3 ? 0x00003F80u : pk2(a1[0], a1[1]), g == 3 ? 0u : pk2(a1[2], a1[3])};
    }
    BARRIER();
    {
      IDS();
      if(layer == 0 && w == 6) {
        for(unsigned spin = 0; spin < SPIN_CAP; ++spin) {
          const v4u p0 = ld16(rws, OFF_PREPG + lane * 32, 0), p1 = ld16(rws, OFF_PREPG + lane * 32 + 16, 0);
          const unsigned tl = (unsigned)tag, th = (unsigned)(tag >> 32);
          if(__all(p0.x == tl && p0.z == tl && p1.x == tl && p1.z == tl && p0.y == th && p0.w == th && p1.y == th && p1.w == th)) break;
          __builtin_amdgcn_s_sleep(1);
          asm volatile("" ::: "memory");
        }
        __builtin_amdgcn_fence(__ATOMIC_ACQUIRE, "wavefront");
        if(b == 0 && lane == 0) set_word(ws, OFF_EPOCH, tag);
      }
      STAMP(6 + layer * 30);
      if(w == 7 && t > 0) wait_flags(ws, ((lane & 1) && t > 1) ? FLAG(t - 2, layer, FL_KV, slot) : FLAG(t - 1, layer, FL_KV, slot), tag);
      BARRIER();
      STAMP(7 + layer * 30);
#pragma unroll
      for(int i = 0; i < 4; ++i) {
        const int pc = i * 8 + w;
        if(16 * t - 32 + pc >= 0) dma1k(rws, lane * 16, PB(t - 2 + (i >> 1), layer) + FO(F_KV + slot * 16 + (pc & 15)), ldsb + L_KV + pc * 1024);
        else *(LAS v4u *)(L + L_KV + pc * 1024 + lane * 16) = v4u{0u, 0u, (g == 3 && !(pc & 1)) ? 0x0000F149u : 0u, 0u};
      }
      f32x4 c0, c16;
#pragma unroll
      for(int r = 0; r < 4; ++r) c0[r] = (4 * g + r > li) ? 0.f : -1e30f, c16[r] = (4 * g + r <= li) ? 0.f : -1e30f;
      DRAIN();
      BARRIER();
      STAMP(8 + layer * 30);
      LAS unsigned char *kv = L + L_KV + w * 2048 + lane * 16;
      const int wb = opq(ID_W2 + CSL(slot) * 224 + w * 28);
      f32x4 s[17];
      {
        v4u kf[17];
#pragma unroll
        for(int i = 0; i < 6; ++i) kf[i] = *(LAS v4u *)(kv + i * 2048);
        SB();
#pragma unroll
        for(int i = 0; i < 17; ++i) {
          if(i + 6 < 17) kf[i + 6] = *(LAS v4u *)(kv + (i + 6) * 2048);
          s[i] = mfma32(kf[i], qf, i == 0 ? c0 : i == 16 ? c16 : f32x4{0.f, 0.f, 0.f, 0.f});
          if((i & 3) == 1 && i < 16)
            dma1k(rws, lane * 16, WT(ID_WO + CS14(slot) * 32 + (i >> 2) * 8 + w), ldsb + L_WO + ((i >> 2) * 8 + w) * 1024);
          SB();
        }
      }
      v4u vf[17];
#pragma unroll
      for(int i = 0; i < 4; ++i) vf[i] = *(LAS v4u *)(kv + i * 2048 + 1024);
      float mx[4] = {-1e30f, -1e30f, -1e30f, -1e30f};
#pragma unroll
      for(int i = 0; i < 17; ++i) {
        mx[(2 * i) & 3] = fmaxf(fmaxf(mx[(2 * i) & 3], s[i][0]), s[i][1]);
        mx[(2 * i + 1) & 3] = fmaxf(fmaxf(mx[(2 * i + 1) & 3], s[i][2]), s[i][3]);
      }
      const float m = gmax(fmaxf(fmaxf(fmaxf(mx[0], mx[1]), mx[2]), mx[3]));
      f32x4 o0 = {0.f, 0.f, 0.f, 0.f}, o1 = {0.f, 0.f, 0.f, 0.f};
#pragma unroll
      for(int i = 0; i < 17; ++i) {
        if(i + 4 < 17) vf[i + 4] = *(LAS v4u *)(kv + (i + 4) * 2048 + 1024);
        const f32x4 d = s[i] - m;
        v2u p;
        p.x = pk2(__builtin_amdgcn_exp2f(d[0]), __builtin_amdgcn_exp2f(d[1]));
        p.y = pk2(__builtin_amdgcn_exp2f(d[2]), __builtin_amdgcn_exp2f(d[3]));
        o0 = mfma16(v2u{vf[i].x, vf[i].y}, p, o0);
        o1 = mfma16(v2u{vf[i].z, vf[i].w}, p, o1);
        if(i < 8 && i * 8 + w < W1_EARLY) dma1k(rws, lane * 16, WT(ID_W1 + slot * 98 + i * 8 + w), ldsb + L_X + (i * 8 + w) * 1024);
        if(layer == 0 && i >= 8 && i < 16) W2LOAD(i - 8);
        if(i & 1) SB();
      }
      const float inv = __builtin_amdgcn_rcpf(__shfl(o1[0], 48 + li));
      o0 *= inv, o1 *= inv;
      const v4u of = {pk2(o0[0], o0[1]), pk2(o0[2], o0[3]), pk2(o1[0], o1[1]), pk2(o1[2], o1[3])};
      st16(of, rws, lane * 16, PB(t, layer) + FO(F_O + w * 16 + slot));
      STAMP(9 + layer * 30);
      DRAIN();
      BARRIER();
      STAMP(10 + layer * 30);
      if(w == 7 && lane == 0) set_word(ws, FLAG(t, layer, FL_O, slot), tag);
    }
    if(SLOT_OF(b0) < 14) {
      IDS();
      if(w == 7) wait_flags(ws, FLAG(t, layer, FL_O, li), tag);
      const rsrc_t rx = layer == 0 ? __builtin_amdgcn_make_buffer_rsrc((void *)x, 0, S * D * 2, 0x00020000) : rws;
      const v4u xr = ld16(rx, layer == 0 ? (16 * tt + li) * (D * 2) + g * 16 : lane * 16,
          layer == 0 ? slot * 64 : PB(t, 2) + FO(F_X2 + w * 16 + slot));
      BARRIER();
      STAMP(12 + layer * 30);
      v4u of[16];
      const int ob = opq(PB(t, layer) + FO(F_O + w * 16));
#pragma unroll
      for(int h = 0; h < 8; ++h) of[h] = ld16(rws, lane * 16 + (h & 3) * 1024, ob + X4(h));
      SB();
      v4u wf[32];
#pragma unroll
      for(int i = 0; i < 8; ++i) wf[i] = *(LAS v4u *)(L + L_WO + i * 1024 + lane * 16);
      SB();
      f32x4 a0 = {0.f, 0.f, 0.f, 0.f}, a1 = {0.f, 0.f, 0.f, 0.f};
#pragma unroll
      for(int h = 0; h < 16; ++h) {
        if(h < 8) of[h + 8] = ld16(rws, lane * 16 + (h & 3) * 1024, ob + X4(h + 8));
        if(h < 12)
          wf[2 * h + 8] = *(LAS v4u *)(L + L_WO + (2 * h + 8) * 1024 + lane * 16),
                     wf[2 * h + 9] = *(LAS v4u *)(L + L_WO + (2 * h + 9) * 1024 + lane * 16);
        a0 = mfma32(wf[2 * h], of[h], a0), a1 = mfma32(wf[2 * h + 1], of[h], a1);
        SB();
      }
      const v4u y = {pk2(a0[0] + bflo(xr.x), a0[1] + bfhi(xr.x)), pk2(a0[2] + bflo(xr.y), a0[3] + bfhi(xr.y)),
          pk2(a1[0] + bflo(xr.z), a1[1] + bfhi(xr.z)), pk2(a1[2] + bflo(xr.w), a1[3] + bfhi(xr.w))};
      st16(y, rws, lane * 16, PB(t, layer) + FO(F_X1 + w * 16 + slot));
      st8(piece_stats(y), rws, lane < 16 ? lane * 8 : OOB, PB(t, 2) + FO(F_ST + (layer * 8 + w) * 2) + slot * 128);
      STAMP(13 + layer * 30);
      DRAIN();
      BARRIER();
      if(w == 7 && lane == 0) set_word(ws, FLAG(t, layer, FL_X1, slot), tag);
    }
    STAMP(14 + layer * 30);
    {
      IDS();
      const int xb = opq(PB(t, layer) + FO(F_X1 + w * 16));
      if(w == 7) wait_flags(ws, FLAG(t, layer, FL_X1, (li < 14 ? li : 0)), tag);
      if(layer == 0 && ti < 112) {
        const int nt = ti >> 4, i = ti & 15;
        float sum = 0.f;
#pragma unroll
        for(int ks = 0; ks < 14; ++ks)
          sum += __uint_as_float(__builtin_amdgcn_raw_buffer_load_b32(rws, OFF_CSP + (ID_W1 + slot * 98 + ks * 7) * 64 + ti * 4, 0, AUX_SC1));
        *(LAS float *)(L + LC_CS1 + ti * 4) = sum;
        const int col = nt < 6 ? 112 * slot + 32 * (nt >> 1) + 8 * (i >> 2) + (i & 3) + 4 * (nt & 1) : 112 * slot + 96 + i;
        *(LAS float *)(L + LC_B1 + ti * 4) = bflo(b1[col]);
      }
      BARRIER();
      STAMP(15 + layer * 30);
      v4u xf[14];
      v2u sp[4];
      ROW_STATS(layer);
#pragma unroll
      for(int ks = 0; ks < XRING; ++ks) XLOAD(ks);
      SB();
      WT_DMA(ID_W1 + slot * 98, W1_EARLY, 98, w, 8, L_X);
      f32x4 acc[7];
#pragma unroll
      for(int n = 0; n < 7; ++n) acc[n] = f32x4{0.f, 0.f, 0.f, 0.f};
      float rs, nmr;
      const int wb = opq(ID_W2 + CSL(slot) * 224 + w * 28);
      SB();
#define STEP_B(ks) \
  if((ks) == 3) {   \
    SUM_STATS(); \
    const float mu = s1 * (1.f / D), var = fmaxf(s2 * (1.f / D) - mu * mu, 0.f), r1 = __builtin_amdgcn_rsqf(var + EPS); \
    rs = r1 * __builtin_amdgcn_rsqf(var * r1 * r1 + EPS), nmr = -mu * rs; \
    asm volatile("" : "+v"(rs), "+v"(nmr)); \
  } \
  STEP_X(ks); \
  if(layer == 0 && (ks) >= 6) { \
    _Pragma("unroll") for(int j = 8 + ((ks)-6) * 3; j < 8 + ((ks)-5) * 3 && j < 28; ++j) W2LOAD(j); \
  }
      GEMM_PART(7, 0, 7, 0, 9, 7, STEP_B);
      asm volatile("s_waitcnt vmcnt(%0)" ::"i"(14 - XRING) : "memory");
      BARRIER();
      GEMM_PART(7, 0, 7, 9, 14, 7, STEP_B);
      STAMP(16 + layer * 30);
      const int ho = PB(t, 0) + FO(F_H + w * 64 + (slot >> 1) * 8);
      f32x4 cc[7], bb[7];
#pragma unroll
      for(int n = 0; n < 7; ++n) cc[n] = *(LAS f32x4 *)(L + LC_CS1 + n * 64 + g * 16), bb[n] = *(LAS f32x4 *)(L + LC_B1 + n * 64 + g * 16);
      SB();
      unsigned hp[14];
#pragma unroll
      for(int n = 0; n < 7; ++n) {
        const f32x4 v = acc[n] * rs + (cc[n] * nmr + bb[n]);
        const f32x4 a = v * (v * v * -0.1029432396f + -2.3022081986f);
        const f32x4 e = {__builtin_amdgcn_exp2f(a[0]), __builtin_amdgcn_exp2f(a[1]), __builtin_amdgcn_exp2f(a[2]), __builtin_amdgcn_exp2f(a[3])};
        const f32x4 d = e + 1.f;
        const f32x4 r = {__builtin_amdgcn_rcpf(d[0]), __builtin_amdgcn_rcpf(d[1]), __builtin_amdgcn_rcpf(d[2]), __builtin_amdgcn_rcpf(d[3])};
        const f32x4 hv = v * r;
        hp[2 * n] = pk2(hv[0], hv[1]), hp[2 * n + 1] = pk2(hv[2], hv[3]);
        if(n & 1)
          st16(v4u{hp[2 * n - 2], hp[2 * n - 1], hp[2 * n], hp[2 * n + 1]}, rws, lane * 16,
              ho + (n < 4 ? 2 * (slot & 1) + (n >> 1) : 4 + (slot & 1)) * 1024);
        if(n == 6) st8(v2u{hp[12], hp[13]}, rws, lane * 16, ho + 6144 + (slot & 1) * 8);
      }
      STAMP(17 + layer * 30);
      DRAIN();
      BARRIER();
      STAMP(18 + layer * 30);
      if(w == 7) {
        if(lane == 0) set_word(ws, FLAG(t, layer, FL_H, slot), tag);
        if(slot < 14) wait_flags(ws, FLAG(t, layer, FL_H, li), tag);
      } else if(layer == 0 && slot < 14) {
        WT_DMA(ID_WQ + slot * 84, 0, WQ_HOP, w, 7, L_X);
      }
    }
    if(SLOT_OF(b0) < 14) {
      IDS();
      const int csl = CSL(slot), chalf = slot >= 7;
      BARRIER();
      STAMP(19 + layer * 30);
      const int ho = opq(PB(t, 0) + FO(F_H + 4 * chalf * 64 + w * 8));
      v4u hf[8];
#pragma unroll
      for(int i = 0; i < 8; ++i) hf[i] = ld16(rws, lane * 16 + (i >> 2) * 1024, ho + (i & 3) * FO(64));
      const int ow = 4 * chalf + (w >> 1), oks = 2 * csl + (w & 1);
      v4u xr, bb;
      SB();
      f32x4 acc[4][4];
#pragma unroll
      for(int q = 0; q < 4; ++q)
#pragma unroll
        for(int n = 0; n < 4; ++n) acc[q][n] = f32x4{0.f, 0.f, 0.f, 0.f};
#pragma unroll
      for(int k = 0; k < 7; ++k) {
#pragma unroll
        for(int q = 0; q < 4; ++q) {
          const v4u cur = hf[(k & 1) * 4 + q];
          if(k + 2 < 7) hf[(k & 1) * 4 + q] = ld16(rws, lane * 16 + (k & 3) * 1024, ho + q * FO(64) + 2048 + (k >> 2) * 4096);
          if(k == 5 && q == 1) xr = ld16(rws, lane * 16, opq(PB(t, layer) + FO(F_X1 + ow * 16 + oks)));
          if(k == 5 && q == 2)
            bb = __builtin_amdgcn_raw_buffer_load_b128(__builtin_amdgcn_make_buffer_rsrc((void *)b2, 0, D * 2, 0x00020000), g * 16, opq(oks * 64), 0);
#pragma unroll
          for(int n = 0; n < 4; ++n) acc[q][n] = mfma32(w2f[k * 4 + n], cur, acc[q][n]);
          SB();
        }
      }
      STAMP(20 + layer * 30);
      if(layer == 0) WT_DMA(ID_WQ + slot * 84, WQ_HOP, 84, w, 8, L_X);
      f32x4 a0, a1;
#pragma unroll
      for(int f = 0; f < 8; ++f) {
        const f32x4 p0 = acc[f >> 1][2 * (f & 1)], p1 = acc[f >> 1][2 * (f & 1) + 1];
        if(f == w) a0 = p0, a1 = p1;
        else
          *(LAS v4u *)(L + L_RED + (f * 7 + (w < f ? w : w - 1)) * 1024 + lane * 16) =
              v4u{pk2(p0[0], p0[1]), pk2(p0[2], p0[3]), pk2(p1[0], p1[1]), pk2(p1[2], p1[3])};
      }
      BARRIER();
      STAMP(21 + layer * 30);
      {
        const unsigned one = g == (li >> 2) ? ((li & 1) ? 0x3F800000u : 0x00003F80u) : 0u;
        const v4u e0 = {(li & 2) ? 0u : one, (li & 2) ? one : 0u, 0u, 0u}, e1 = {0u, 0u, (li & 2) ? 0u : one, (li & 2) ? one : 0u};
        v4u p[7];
#pragma unroll
        for(int s = 0; s < 7; ++s) p[s] = *(LAS v4u *)(L + L_RED + (w * 7 + s) * 1024 + lane * 16);
        a0 = mfma32(e0, xr, a0), a1 = mfma32(e1, xr, a1);
        a0 = mfma32(e0, bb, a0), a1 = mfma32(e1, bb, a1);
#pragma unroll
        for(int s = 0; s < 7; ++s) a0 = mfma32(e0, p[s], a0), a1 = mfma32(e1, p[s], a1);
      }
      const v4u y = {pk2(a0[0], a0[1]), pk2(a0[2], a0[3]), pk2(a1[0], a1[1]), pk2(a1[2], a1[3])};
      if(layer == 0) {
        st16(y, rws, lane * 16, PB(t, 2) + FO(F_X2 + ow * 16 + oks));
        st8(piece_stats(y), rws, lane < 16 ? lane * 8 : OOB, PB(t, 2) + FO(F_ST + (16 + ow) * 2) + oks * 128);
        STAMP(22 + layer * 30);
      } else {
        *(GAS v4u *)(out + (16 * (8 * t + ow) + li) * D + 32 * oks + 8 * g) = y;
      }
    }
    STAMP(23 + layer * 30);
    if(layer == 0) {
      IDS();
      if(slot >= 14) WT_DMA(ID_WQ + slot * 84, 0, 84, w, 8, L_X);
      DRAIN();
      BARRIER();
      if(w == 7) {
        if(lane == 0 && slot < 14) set_word(ws, FLAG(t, 0, FL_X2, slot), tag);
        wait_flags(ws, FLAG(t, 0, FL_X2, (li < 14 ? li : 0)), tag);
      }
      BARRIER();
    }
  };
  run_layer(IntC<0>{});
  run_layer(IntC<1>{});
  STAMP(61);
  asm volatile(".Lfk_end:");
}
}

extern "C" void kernel_launch(void *const *d_in, const int *in_sizes, int n_in, void *d_out, int out_size, void *d_ws, size_t ws_size,
    hipStream_t stream) {
  const size_t pad = (size_t)(-(intptr_t)d_ws) & (size_t)0x1FFFFF;
  fused_kernel<<<NWG, NTHR, 0, stream>>>((const u16 *)d_in[0], (const u16 *)d_in[1], (const u16 *)d_in[2], (const u16 *)d_in[4],
      (const u16 *)d_in[5], (const u16 *)d_in[6], (const u16 *)d_in[7], (u16 *)d_out, (unsigned char *)d_ws + pad);
}
```

```cpp
#include <hip/hip_runtime.h>
#include <stdint.h>

#define GAS __attribute__((address_space(1)))
#define LAS __attribute__((address_space(3)))
#define DI __device__ __forceinline__
typedef unsigned v4u __attribute__((ext_vector_type(4)));
typedef unsigned v2u __attribute__((ext_vector_type(2)));
typedef float f32x4 __attribute__((ext_vector_type(4)));
typedef float f32x2 __attribute__((ext_vector_type(2)));
typedef __bf16 bf16x8 __attribute__((ext_vector_type(8)));
typedef __bf16 bf16x2 __attribute__((ext_vector_type(2)));
typedef short s16x4 __attribute__((ext_vector_type(4)));
typedef unsigned long long u64;
typedef unsigned short u16;
typedef __amdgpu_buffer_rsrc_t rsrc_t;

#define TILE_OF(b) ((((b)&7) >> 1) * 4 + ((b) >> 6))
#define SLOT_OF(b) (((b)&1) * 8 + (((b) >> 3) & 7))
#define CS14(s) ((s) >= 14 ? 0 : (s))
#define CSL(s) ((s) >= 14 ? 0 : (s) >= 7 ? (s)-7 : (s))

namespace {
template <int V> struct IntC {
  static constexpr int value = V;
};
constexpr int S = 2048, D = 448, F = 1792;
constexpr int NWG = 256, NTHR = 512;
constexpr float EPS = 1e-5f;
constexpr int AUX_SC1 = 16;
constexpr unsigned SPIN_CAP = 1u << 16;
constexpr int OOB = 0x7F000000;
constexpr int XRING = 8;
constexpr int WDEP = 7;
constexpr u64 VOTE = 1ull << 63;

constexpr int FS = 128;
constexpr int OFF_EPOCH = 0, OFF_PREPG = 4096, OFF_PREPF = 8192;
constexpr int OFF_CSP = 1 << 20;
constexpr int OFF_WT = 2 << 20;
constexpr int ID_WO = 0, ID_W1 = 448, ID_W2 = 2016, ID_WQ = 3584, ID_END = 4928;
constexpr int OFF_PROBE = 12 << 20;
constexpr int PG0 = 8, WS_BYTES = (PG0 + 48) << 21;
constexpr int F_KV = 0, F_O = 256, F_X1 = 384, F_H = 512, F_X2 = 0, F_ST = 128, F_FL = 192;
constexpr int FL_KV = 0, FL_O = 1, FL_X1 = 2, FL_H = 3, FL_X2 = 4;
#define FO(f) ((((f) >> 3) << 14) + (((f)&7) << 10))
#define PB(t_, p_) (((PG0 + 3 * (t_) + (p_)) << 21) + (((side ^ (t_) ^ (p_) ^ ((t_) >> 3)) & 1) << 13))
#define FLAG(t_, layer_, ph_, i_) (PB(t_, 2) + FO(F_FL + ((layer_)*5 + (ph_)) * 2) + (i_)*FS)
#define WT(f) (OFF_WT + (f)*1024)
static_assert(OFF_CSP + ID_END * 64 <= OFF_WT && OFF_WT + ID_END * 1024 <= OFF_PROBE && (PG0 & 1) == 0, "map");

constexpr int L_X = 0;
constexpr int L_KV = 100352 - 32768;
constexpr int L_WO = 100352 + 16384;
constexpr int L_RED = 86016;
constexpr int L_C = L_WO + 32768;
constexpr int LC_CSQ = L_C, LC_CS1 = L_C + 512, LC_B1 = L_C + 1024;
constexpr int LS_Q = L_C + 1536, LS_HOLD = LS_Q + 4, LS_KV = LS_Q + 8, LS_SIDE = LS_Q + 32;
constexpr int LDS_BYTES = L_C + 2048;
constexpr int W1_EARLY = 63;
constexpr int WQ_HOP = 28;
static_assert(W1_EARLY * 1024 <= L_KV && L_RED + 56 * 1024 <= L_C, "LDS map");

DI f32x4 mfma32(v4u a, v4u b, f32x4 c) {
  return __builtin_amdgcn_mfma_f32_16x16x32_bf16(__builtin_bit_cast(bf16x8, a), __builtin_bit_cast(bf16x8, b), c, 0, 0, 0);
}
DI f32x4 mfma16(v2u a, v2u b, f32x4 c) {
  return __builtin_amdgcn_mfma_f32_16x16x16bf16_1k(__builtin_bit_cast(s16x4, a), __builtin_bit_cast(s16x4, b), c, 0, 0, 0);
}
DI unsigned pk2(float a, float b) {
  f32x2 v = {a, b};
  return __builtin_bit_cast(unsigned, __builtin_convertvector(v, bf16x2));
}
DI float bflo(unsigned u) { return __uint_as_float(u << 16); }
DI float bfhi(unsigned u) { return __uint_as_float(u & 0xFFFF0000u); }
DI float dot2(unsigned a, unsigned b, float c) {
  return __builtin_amdgcn_fdot2_f32_bf16(__builtin_bit_cast(bf16x2, a), __builtin_bit_cast(bf16x2, b), c, false);
}
DI float gsum(float x) {
  auto r = __builtin_amdgcn_permlane32_swap(__float_as_uint(x), __float_as_uint(x), false, false);
  x = __uint_as_float(r[0]) + __uint_as_float(r[1]);
  auto q = __builtin_amdgcn_permlane16_swap(__float_as_uint(x), __float_as_uint(x), false, false);
  return __uint_as_float(q[0]) + __uint_as_float(q[1]);
}
DI float gmax(float x) {
  auto r = __builtin_amdgcn_permlane32_swap(__float_as_uint(x), __float_as_uint(x), false, false);
  x = fmaxf(fmaxf(x, __uint_as_float(r[0])), __uint_as_float(r[1]));
  auto q = __builtin_amdgcn_permlane16_swap(__float_as_uint(x), __float_as_uint(x), false, false);
  return fmaxf(fmaxf(x, __uint_as_float(q[0])), __uint_as_float(q[1]));
}
DI v4u ld16(rsrc_t rs, int voff, int soff) { return __builtin_amdgcn_raw_buffer_load_b128(rs, voff, soff, AUX_SC1); }
DI v2u ld8(rsrc_t rs, int voff, int soff) { return __builtin_amdgcn_raw_buffer_load_b64(rs, voff, soff, AUX_SC1); }
DI void st16(v4u v, rsrc_t rs, int voff, int soff) {
  __builtin_amdgcn_raw_buffer_store_b128(v, rs, voff, soff, AUX_SC1);
  asm volatile("s_nop 1" ::"v"(v) : "memory");
}
DI void st8(v2u v, rsrc_t rs, int voff, int soff) { __builtin_amdgcn_raw_buffer_store_b64(v, rs, voff, soff, AUX_SC1); }
DI int opq(int v) {
  asm volatile("" : "+s"(v));
  return v;
}
DI int opqv(int v) {
  asm volatile("" : "+v"(v));
  return v;
}
DI v2u trread(LAS unsigned char *p) { return __builtin_bit_cast(v2u, __builtin_amdgcn_ds_read_tr16_b64_v4i16((LAS s16x4 *)p)); }
#define DRAIN() asm volatile("s_waitcnt vmcnt(0)" ::: "memory")
#define BARRIER() asm volatile("s_waitcnt lgkmcnt(0)\n\ts_barrier" ::: "memory")
#define SB() __builtin_amdgcn_sched_barrier(0)
#define dma1k(rs, voff, soff, ldsaddr) \
  asm volatile("s_mov_b32 m0, %0\n\ts_nop 0\n\tbuffer_load_dwordx4 %1, %2, %3 offen sc1 lds" ::"s"((unsigned)(ldsaddr)), "v"((int)(voff)), \
               "s"(rs), "s"((int)(soff)) \
               : "memory")
#define dma1k_input(rs, voff, soff, ldsaddr) \
  asm volatile("s_mov_b32 m0, %0\n\ts_nop 0\n\tbuffer_load_dwordx4 %1, %2, %3 offen lds" ::"s"((unsigned)(ldsaddr)), "v"((int)(voff)), \
               "s"(rs), "s"((int)(soff)) \
               : "memory")
DI void wait_flags(const unsigned char *ws, int off, u64 tag) {
  const GAS u64 *p = (const GAS u64 *)(ws + off);
  for(unsigned spin = 0; spin < SPIN_CAP; ++spin) {
    const u64 v = __hip_atomic_load(p, __ATOMIC_RELAXED, __HIP_MEMORY_SCOPE_AGENT);
    if(__all(v == tag)) break;
    __builtin_amdgcn_s_sleep(1);
  }
  __builtin_amdgcn_fence(__ATOMIC_ACQUIRE, "wavefront");
}
DI void set_word(unsigned char *ws, int off, u64 v) {
  __hip_atomic_store((GAS u64 *)(ws + off), v, __ATOMIC_RELAXED, __HIP_MEMORY_SCOPE_AGENT);
}
DI unsigned lds_count(LAS unsigned char *p, unsigned add, int lane) {
  unsigned old = 0;
  if(lane == 0) old = __hip_atomic_fetch_add((LAS unsigned *)p, add, __ATOMIC_RELAXED, __HIP_MEMORY_SCOPE_WORKGROUP);
  return __builtin_amdgcn_readfirstlane(old);
}
DI v2u piece_stats(v4u y) {
  float s1 = 0.f, s2 = 0.f;
#pragma unroll
  for(int j = 0; j < 4; ++j) s1 = dot2(y[j], 0x3F803F80u, s1), s2 = dot2(y[j], y[j], s2);
  return v2u{__float_as_uint(gsum(s1)), __float_as_uint(gsum(s2))};
}

__global__ void __launch_bounds__(NTHR, 2)
    fused_kernel(const u16 *__restrict__ x, const u16 *__restrict__ wqkv, const u16 *__restrict__ wo, const u16 *__restrict__ w1,
        const u16 *__restrict__ b1, const u16 *__restrict__ w2, const u16 *__restrict__ b2, u16 *__restrict__ out, unsigned char *ws) {
  __shared__ __attribute__((aligned(16))) unsigned char smem[LDS_BYTES];
  LAS unsigned char *L = (LAS unsigned char *)smem;
  const unsigned ldsb = (unsigned)(size_t)L;
  const int tid = threadIdx.x, w0 = __builtin_amdgcn_readfirstlane(tid >> 6), b0 = blockIdx.x;
#define IDS() \
  const int ti = opqv(tid), lane = ti & 63, w = opq(w0), b = opq(b0), t = TILE_OF(b), slot = SLOT_OF(b); \
  const int tt = 8 * t + w, g = lane >> 4, li = lane & 15; \
  (void)g, (void)li, (void)tt, (void)slot
#define WT_DMA(id0, from, to, w_, nw_, ldsoff) \
  for(int i_ = (from) + (w_); i_ < (to); i_ += (nw_)) dma1k(rws, lane * 16, WT((id0) + i_), ldsb + (ldsoff) + i_ * 1024)
#define GEMM_PART(NT, N0, NN, K0, K1, SWAPN, STEP) \
  { \
    constexpr int CNT = ((K1) - (K0)) * (NN); \
    v4u wr[WDEP]; \
    _Pragma("unroll") for(int i = 0; i < WDEP; ++i) wr[i] = *(LAS v4u *)(L + L_X + (((K0) + i / (NN)) * (NT) + (N0) + i % (NN)) * 1024 + lane * 16); \
    SB(); \
    _Pragma("unroll") for(int i = 0; i < CNT; ++i) { \
      const int ks = (K0) + i / (NN), n = (N0) + i % (NN); \
      if(i % (NN) == 0) { \
        STEP(ks); \
      } \
      const v4u cur = wr[i % WDEP]; \
      if(i + WDEP < CNT) \
        wr[i % WDEP] = *(LAS v4u *)(L + L_X + (((K0) + (i + WDEP) / (NN)) * (NT) + (N0) + (i + WDEP) % (NN)) * 1024 + lane * 16); \
      acc[n] = n < (SWAPN) ? mfma32(cur, xf[ks], acc[n]) : mfma32(xf[ks], cur, acc[n]); \
      SB(); \
    } \
  }
#define X4(k) ((((k) >> 3) << 14) + ((((k) >> 2) & 1) << 12))
#define XLOAD(k) xf[k] = ld16(rws, lane * 16 + ((k)&3) * 1024, xb + X4(k))
#define STEP_NONE(ks)
#define STEP_X(ks) \
  if((ks) + XRING < 14) XLOAD((ks) + XRING)
#define ROW_STATS(which) \
  _Pragma("unroll") for(int i = 0; i < 4; ++i) \
      sp[i] = ld8(rws, (i == 3 && g > 1) ? OOB : li * 8 + g * 128 + i * 512, PB(t, 2) + FO(F_ST + ((which)*8 + w) * 2))
#define SUM_STATS() \
  const float s1 = gsum((__uint_as_float(sp[0].x) + __uint_as_float(sp[1].x)) + (__uint_as_float(sp[2].x) + __uint_as_float(sp[3].x))); \
  const float s2 = gsum((__uint_as_float(sp[0].y) + __uint_as_float(sp[1].y)) + (__uint_as_float(sp[2].y) + __uint_as_float(sp[3].y)))
#define W2LOAD(j) w2f[j] = ld16(rws, lane * 16 + ((j)&3) * 1024, WT(wb + ((j) & ~3)))
  const rsrc_t rws = __builtin_amdgcn_make_buffer_rsrc((void *)ws, 0, WS_BYTES, 0x00020000);
  u64 tag = 0;
  int side = 0;
  {
    u64 pc;
    int len;
    asm volatile("s_getpc_b64 %0\n.Lfk_here:\n\ts_mov_b32 %1, .Lfk_end-.Lfk_here" : "=s"(pc), "=s"(len));
    if(w0 == 7) {
      const rsrc_t rc = __builtin_amdgcn_make_buffer_rsrc((void *)pc, 0, len, 0x00020000);
      asm volatile("s_nop 4" ::: "memory");
      dma1k_input(rc, (tid & 63) * 16, (b0 >> 3) * 2048, ldsb + L_WO + 30720);
      dma1k_input(rc, (tid & 63) * 16, (b0 >> 3) * 2048 + 1024, ldsb + L_WO + 31744);
    }
    if(w0 == 6)
      asm volatile("s_mov_b32 m0, %0\n\ts_nop 0\n\tbuffer_load_dword %1, %2, 0 offen sc1 lds" ::"s"(ldsb + L_WO + 28672),
                   "v"((tid & 63) < 9 ? (max(PG0 + 3 * (TILE_OF(b0) - 2) + (tid & 63), 0) << 21) + 0x1FD000 + (b0 & 31) * 128 : OOB), "s"(rws)
                   : "memory");
  }
  v4u w2f[28];

  auto run_layer = [&](auto LC) __attribute__((always_inline)) {
    constexpr int layer = decltype(LC)::value;
    v4u qf;
    {
      IDS();
      v4u xf[14];
      float mu, rs, mur[4], rsr[4];
      f32x4 acc[6];
#pragma unroll
      for(int n = 0; n < 6; ++n) acc[n] = f32x4{0.f, 0.f, 0.f, 0.f};
#define A_FINISH_STATS() \
  mu = s1 * (1.f / D), rs = __builtin_amdgcn_rsqf(fmaxf(s2 * (1.f / D) - mu * mu, 0.f) + EPS); \
  _Pragma("unroll") for(int r = 0; r < 4; ++r) mur[r] = __shfl(mu, 4 * g + r), rsr[r] = __shfl(rs, 4 * g + r); \
  asm volatile("" : "+v"(mu), "+v"(rs), "+v"(mur[0]), "+v"(mur[1]), "+v"(mur[2]), "+v"(mur[3]), "+v"(rsr[0]), "+v"(rsr[1]), "+v"(rsr[2]), "+v"(rsr[3]))
      if(layer == 0) {
        int dst[2], dst1[2], tro[2], tre[2];
        unsigned char *pp = ws + OFF_PROBE + (b & 31) * 16384 + w * 512;
        if(w < 3 && b >= 224) {
          asm volatile("global_store_dword %0, %1, off sc1" ::"v"(pp + (lane & 1) * 8192 + ((lane >> 1) & 1) * 128), "v"(0u) : "memory");
        } else if(w < 3) {
          const int r = lane >> 2, c = lane & 3, head = b / 14, ks = b - 14 * head;
          const rsrc_t rq = __builtin_amdgcn_make_buffer_rsrc((void *)wqkv, 0, D * 3 * D * 2, 0x00020000);
          const int o0 = (32 * ks + r) * 2688 + w * 896 + 56 * head + c * 16;
          dma1k_input(rq, o0, 0, ldsb + L_WO + w * 2048);
          dma1k_input(rq, o0, 16 * 2688, ldsb + L_WO + w * 2048 + 1024);
          dst[0] = ID_WQ + head * 84 + ks * 6 + w * 2, dst1[0] = dst[0] + 1, tro[0] = 16, tre[0] = 8;
        }
        v2u ep;
        asm volatile("buffer_load_dwordx2 %0, off, %1, 0 sc1" : "=v"(ep) : "s"(rws) : "memory");
        if(ti < 8) *(LAS unsigned *)(L + LS_Q + 4 * ti) = 0u;
        const rsrc_t rx = __builtin_amdgcn_make_buffer_rsrc((void *)x, 0, S * D * 2, 0x00020000);
        const int xvo = (16 * tt + (lane >> 3)) * (D * 2) + (((lane & 7) ^ (lane >> 3)) * 16);
#define X0_DMA() \
  _Pragma("unroll") for(int i = 0; i < 14; ++i) dma1k_input(rx, xvo, (i & 1) * 8 * (D * 2) + (i >> 1) * 128, ldsb + w * 14336 + i * 1024)
#define TAKE_TAG() \
  asm volatile("s_waitcnt vmcnt(0)" : "+v"(ep)::"memory"); \
  tag = (((u64)(unsigned)__builtin_amdgcn_readfirstlane(ep.y) << 32) | (unsigned)__builtin_amdgcn_readfirstlane(ep.x)) + 1ull
#define PREP_OUT(rd, QPAD) \
  { \
    LAS unsigned char *p = L + L_WO + ((rd)*8 + w) * 2048 + (8 * g + (li >> 2)) * 64 + (li & 3) * tro[rd]; \
    const v2u a0 = trread(p), a1 = trread(p + 256), c0 = trread(p + tre[rd]), c1 = trread(p + tre[rd] + 256); \
    const v4u f0 = {a0.x, a0.y, a1.x, a1.y}; \
    v4u f1 = {c0.x, c0.y, c1.x, c1.y}; \
    if(QPAD && li >= 12) f1 = v4u{0u, 0u, 0u, 0u}; \
    const bool ok = dst[rd] < ID_END; \
    st16(f0, rws, ok ? lane * 16 : OOB, WT(dst[rd])); \
    st16(f1, rws, ok ? lane * 16 : OOB, WT(dst1[rd])); \
    float ca = 0.f, cb = 0.f; \
    _Pragma("unroll") for(int j = 0; j < 4; ++j) ca = dot2(f0[j], 0x3F803F80u, ca), cb = dot2(f1[j], 0x3F803F80u, cb); \
    ca = gsum(ca), cb = gsum(cb); \
    __builtin_amdgcn_raw_buffer_store_b32( \
        __float_as_uint(g ? cb : ca), rws, (ok && g < 2) ? OFF_CSP + (g ? dst1[rd] : dst[rd]) * 64 + li * 4 : OOB, 0, AUX_SC1); \
  }
        BARRIER();
        if(w < 3) {
          unsigned vote = 1u;
          TAKE_TAG();
          if(b >= 224) {
            unsigned v;
            unsigned best[2] = {~0u, ~0u};
#pragma unroll
            for(int i = 0; i < 4; ++i) {
              u64 t0, t1;
              asm volatile(
                  "s_memtime %0\n\ts_waitcnt lgkmcnt(0)\n\tglobal_load_dword %2, %3, off sc1\n\ts_waitcnt vmcnt(0)\n\ts_memtime %1\n\ts_waitcnt lgkmcnt(0)"
                  : "=&s"(t0), "=&s"(t1), "=&v"(v)
                  : "v"(pp + ((i ^ w ^ (b >> 3)) & 1) * 8192 + (i >> 1) * 128)
                  : "memory");
              const unsigned dt = (unsigned)(t1 - t0);
              if((i ^ w ^ (b >> 3)) & 1) best[1] = min(best[1], dt);
              else best[0] = min(best[0], dt);
            }
            if((best[1] < best[0]) != (bool)((b >> 2) & 1)) vote = 0x101u;
          } else {
            PREP_OUT(0, true);
          }
          lds_count(L + LS_HOLD, 1u, lane);
          DRAIN();
          const unsigned cnt = lds_count(L + LS_Q, vote, lane) + vote;
          if((cnt & 0xFFu) == 3u && lane == 0) set_word(ws, OFF_PREPF + b * FS, cnt >= 0x200u ? tag ^ VOTE : tag);
          X0_DMA();
          DRAIN();
        } else {
#pragma unroll
          for(int rd = 0; rd < 2; ++rd) {
            const int id = rd == 0 ? b * 5 + (w - 3) : w < 5 ? 1280 + b * 2 + (w - 3) : 1792, r = lane >> 2, c = lane & 3;
            const u16 *src;
            int bytes, o0, o1;
            tro[rd] = 16, tre[rd] = 8;
            if(id < 224) {
              const int ks = id / 14, sl = id - ks * 14;
              src = wo, bytes = D * D * 2;
              o0 = (28 * ks + r) * 896 + 64 * sl + c * 16, o1 = r < 12 ? o0 + 16 * 896 : OOB;
              dst[rd] = ID_WO + sl * 32 + ks * 2, dst1[rd] = dst[rd] + 1;
            } else if(id < 224 + 672) {
              const int i2 = id - 224, ks = i2 / 48, rem = i2 - ks * 48, sl = rem / 3, p = rem - sl * 3;
              src = w1, bytes = D * F * 2;
              o0 = (32 * ks + r) * 3584 + 224 * sl + 64 * p + c * 16, o1 = o0 + 16 * 3584;
              dst[rd] = ID_W1 + sl * 98 + ks * 7 + 2 * p, dst1[rd] = dst[rd] + 1;
            } else if(id < 896 + 112) {
              const int i2 = id - 896, ks = i2 >> 3, sl = 2 * (i2 & 7);
              src = w1, bytes = D * F * 2;
              o0 = (32 * ks + r) * 3584 + 224 * (sl + (c >> 1)) + 192 + (c & 1) * 16, o1 = o0 + 16 * 3584;
              dst[rd] = ID_W1 + sl * 98 + ks * 7 + 6, dst1[rd] = dst[rd] + 98, tro[rd] = 8, tre[rd] = 32;
            } else {
              const int i2 = id - 1008, hks = i2 / 14, cb = i2 - hks * 14, wv = hks / 7, kk = hks - 7 * wv;
              src = w2, bytes = F * D * 2;
              const int row = 224 * wv +
                  (kk < 4 ? 112 * (kk >> 1) + 32 * (kk & 1) + r : kk < 6 ? 112 * (kk - 4) + 64 + r : 96 + 112 * ((r >> 2) & 1) + 4 * (r >> 3) + (r & 3));
              o0 = row * 896 + 64 * cb + c * 16, o1 = o0 + (kk < 6 ? 16 : 8) * 896;
              dst[rd] = ID_W2 + (cb >> 1) * 224 + hks * 4 + (cb & 1) * 2, dst1[rd] = dst[rd] + 1;
              if(id >= 1792) o0 = o1 = OOB, dst[rd] = dst1[rd] = ID_END;
            }
            const rsrc_t rsw = __builtin_amdgcn_make_buffer_rsrc((void *)src, 0, bytes, 0x00020000);
            asm volatile("s_nop 4" ::: "memory");
            dma1k_input(rsw, o0, 0, ldsb + L_WO + (rd * 8 + w) * 2048);
            dma1k_input(rsw, o1, 0, ldsb + L_WO + (rd * 8 + w) * 2048 + 1024);
          }
          for(unsigned spin = 0; spin < (1u << 20); ++spin)
            if(__builtin_amdgcn_readfirstlane(__hip_atomic_load((LAS unsigned *)(L + LS_HOLD), __ATOMIC_RELAXED, __HIP_MEMORY_SCOPE_WORKGROUP)) >= 3)
              break;
          X0_DMA();
          TAKE_TAG();
          if(w == 7) wait_flags(ws, OFF_PREPF + (slot * 14 + (li < 14 ? li : 0)) * FS, tag);
        }
#pragma unroll
        for(int ks = 0; ks < 14; ++ks)
          xf[ks] = *(LAS v4u *)(L + w * 14336 + ((ks >> 1) * 2 + (li >> 3)) * 1024 + (li & 7) * 128 + (((ks & 1) * 4 + g) ^ (li & 7)) * 16);
        BARRIER();
        WT_DMA(ID_WQ + slot * 84, 0, 84, w, 8, L_X);
        {
          float s1 = 0.f, s2 = 0.f, s3 = 0.f, s4 = 0.f;
#pragma unroll
          for(int ks = 0; ks < 14; ++ks) {
            s1 = dot2(xf[ks][0], 0x3F803F80u, s1), s2 = dot2(xf[ks][0], xf[ks][0], s2);
            s3 = dot2(xf[ks][1], 0x3F803F80u, s3), s4 = dot2(xf[ks][1], xf[ks][1], s4);
            s1 = dot2(xf[ks][2], 0x3F803F80u, s1), s2 = dot2(xf[ks][2], xf[ks][2], s2);
            s3 = dot2(xf[ks][3], 0x3F803F80u, s3), s4 = dot2(xf[ks][3], xf[ks][3], s4);
          }
          s1 = gsum(s1 + s3), s2 = gsum(s2 + s4);
          A_FINISH_STATS();
        }
        unsigned cv[14];
        if(w >= 3) {
          PREP_OUT(0, false);
          PREP_OUT(1, false);
          asm volatile("s_waitcnt vmcnt(11)" ::: "memory");
        } else {
          asm volatile("s_waitcnt vmcnt(5)" ::: "memory");
#pragma unroll
          for(int ks = 0; ks < 14; ++ks)
            cv[ks] = __builtin_amdgcn_raw_buffer_load_b32(rws, ti < 96 ? OFF_CSP + (ID_WQ + slot * 84 + ks * 6) * 64 + ti * 4 : OOB, 0, AUX_SC1);
          SB();
        }
        BARRIER();
        GEMM_PART(6, 2, 4, 0, 7, 4, STEP_NONE);
        if(ti < 96) {
          float csum = 0.f;
#pragma unroll
          for(int ks = 0; ks < 14; ++ks) csum += __uint_as_float(cv[ks]);
          *(LAS float *)(L + LC_CSQ + ti * 4) = csum;
        }
        if(w == 2) {
          u64 v = 0;
          for(unsigned spin = 0; spin < SPIN_CAP; ++spin) {
            v = __hip_atomic_load((const GAS u64 *)(ws + OFF_PREPF + (224 + (lane & 31)) * FS), __ATOMIC_RELAXED, __HIP_MEMORY_SCOPE_AGENT);
            if(__all((v | VOTE) == (tag | VOTE))) break;
          }
          const int ones = __popcll(__ballot(v != tag));
          if(lane == 0) *(LAS int *)(L + LS_SIDE) = ones > 32;
        }
        DRAIN();
        BARRIER();
        side = __builtin_amdgcn_readfirstlane(*(LAS int *)(L + LS_SIDE));
        if(ti == 0) set_word(ws, OFF_PREPG + b * 8, tag);
        GEMM_PART(6, 2, 4, 7, 14, 4, STEP_NONE);
      } else {
        const int xb = opq(PB(t, 2) + FO(F_X2 + w * 16));
        v2u sp[4];
        ROW_STATS(2);
#pragma unroll
        for(int ks = 0; ks < XRING; ++ks) XLOAD(ks);
        SB();
#define STEP_XA(ks) \
  if((ks) == 3) { \
    SUM_STATS(); \
    A_FINISH_STATS(); \
  } \
  STEP_X(ks)
        GEMM_PART(6, 2, 4, 0, 14, 4, STEP_XA);
      }
      const float qs = rs * (0.18898223650461362f * 1.4426950408889634f);
      f32x4 c[4];
#pragma unroll
      for(int n = 0; n < 4; ++n) c[n] = *(LAS f32x4 *)(L + LC_CSQ + n * 64 + g * 16);
      const float c4 = *(LAS float *)(L + LC_CSQ + 256 + li * 4), c5 = *(LAS float *)(L + LC_CSQ + 320 + li * 4);
      f32x4 a0 = (acc[2] - mu * c[2]) * rs, a1 = (acc[3] - mu * c[3]) * rs;
      const v4u kf = {pk2(a0[0], a0[1]), pk2(a0[2], a0[3]), g == 3 ? 0u : pk2(a1[0], a1[1]), g == 3 ? 0u : pk2(a1[2], a1[3])};
#pragma unroll
      for(int r = 0; r < 4; ++r) a0[r] = (acc[4][r] - mur[r] * c4) * rsr[r], a1[r] = (acc[5][r] - mur[r] * c5) * rsr[r];
      const unsigned vpad = li == 12 ? 0x3F803F80u : 0u;
      const v4u vf = {pk2(a0[0], a0[1]), pk2(a0[2], a0[3]), li >= 12 ? vpad : pk2(a1[0], a1[1]), li >= 12 ? vpad : pk2(a1[2], a1[3])};
      const int kvo = PB(t, layer) + FO(F_KV + (slot * 8 + w) * 2);
      st16(kf, rws, lane * 16, kvo);
      st16(vf, rws, lane * 16 + 1024, kvo);
      *(LAS v4u *)(L + L_KV + (16 + w) * 2048 + lane * 16) = kf;
      *(LAS v4u *)(L + L_KV + (16 + w) * 2048 + 1024 + lane * 16) = vf;
#define STEP_KVF(ks) \
  if((ks) == 9) { \
    DRAIN(); \
    if(lds_count(L + LS_KV, 1u, lane) == 8u * (layer + 1) - 1u && lane == 0) set_word(ws, FLAG(t, layer, FL_KV, slot), tag); \
    SB(); \
  }
      GEMM_PART(6, 0, 2, 0, 14, 4, STEP_KVF);
      a0 = (acc[0] - mu * c[0]) * qs, a1 = (acc[1] - mu * c[1]) * qs;
      qf = v4u{pk2(a0[0], a0[1]), pk2(a0[2], a0[3]), g == 3 ? 0x00003F80u : pk2(a1[0], a1[1]), g == 3 ? 0u : pk2(a1[2], a1[3])};
    }
    BARRIER();
    {
      IDS();
      if(layer == 0 && w == 6) {
        for(unsigned spin = 0; spin < SPIN_CAP; ++spin) {
          const v4u p0 = ld16(rws, OFF_PREPG + lane * 32, 0), p1 = ld16(rws, OFF_PREPG + lane * 32 + 16, 0);
          const unsigned tl = (unsigned)tag, th = (unsigned)(tag >> 32);
          if(__all(p0.x == tl && p0.z == tl && p1.x == tl && p1.z == tl && p0.y == th && p0.w == th && p1.y == th && p1.w == th)) break;
          __builtin_amdgcn_s_sleep(1);
          asm volatile("" ::: "memory");
        }
        __builtin_amdgcn_fence(__ATOMIC_ACQUIRE, "wavefront");
        if(b == 0 && lane == 0) set_word(ws, OFF_EPOCH, tag);
      }
      if(w == 7 && t > 0) wait_flags(ws, ((lane & 1) && t > 1) ? FLAG(t - 2, layer, FL_KV, slot) : FLAG(t - 1, layer, FL_KV, slot), tag);
      BARRIER();
#pragma unroll
      for(int i = 0; i < 4; ++i) {
        const int pc = i * 8 + w;
        if(16 * t - 32 + pc >= 0) dma1k(rws, lane * 16, PB(t - 2 + (i >> 1), layer) + FO(F_KV + slot * 16 + (pc & 15)), ldsb + L_KV + pc * 1024);
        else *(LAS v4u *)(L + L_KV + pc * 1024 + lane * 16) = v4u{0u, 0u, (g == 3 && !(pc & 1)) ? 0x0000F149u : 0u, 0u};
      }
      f32x4 c0, c16;
#pragma unroll
      for(int r = 0; r < 4; ++r) c0[r] = (4 * g + r > li) ? 0.f : -1e30f, c16[r] = (4 * g + r <= li) ? 0.f : -1e30f;
      DRAIN();
      BARRIER();
      LAS unsigned char *kv = L + L_KV + w * 2048 + lane * 16;
      const int wb = opq(ID_W2 + CSL(slot) * 224 + w * 28);
      f32x4 s[17];
      {
        v4u kf[17];
#pragma unroll
        for(int i = 0; i < 6; ++i) kf[i] = *(LAS v4u *)(kv + i * 2048);
        SB();
#pragma unroll
        for(int i = 0; i < 17; ++i) {
          if(i + 6 < 17) kf[i + 6] = *(LAS v4u *)(kv + (i + 6) * 2048);
          s[i] = mfma32(kf[i], qf, i == 0 ? c0 : i == 16 ? c16 : f32x4{0.f, 0.f, 0.f, 0.f});
          if((i & 3) == 1 && i < 16)
            dma1k(rws, lane * 16, WT(ID_WO + CS14(slot) * 32 + (i >> 2) * 8 + w), ldsb + L_WO + ((i >> 2) * 8 + w) * 1024);
          SB();
        }
      }
      v4u vf[17];
#pragma unroll
      for(int i = 0; i < 4; ++i) vf[i] = *(LAS v4u *)(kv + i * 2048 + 1024);
      float mx[4] = {-1e30f, -1e30f, -1e30f, -1e30f};
#pragma unroll
      for(int i = 0; i < 17; ++i) {
        mx[(2 * i) & 3] = fmaxf(fmaxf(mx[(2 * i) & 3], s[i][0]), s[i][1]);
        mx[(2 * i + 1) & 3] = fmaxf(fmaxf(mx[(2 * i + 1) & 3], s[i][2]), s[i][3]);
      }
      const float m = gmax(fmaxf(fmaxf(fmaxf(mx[0], mx[1]), mx[2]), mx[3]));
      f32x4 o0 = {0.f, 0.f, 0.f, 0.f}, o1 = {0.f, 0.f, 0.f, 0.f};
#pragma unroll
      for(int i = 0; i < 17; ++i) {
        if(i + 4 < 17) vf[i + 4] = *(LAS v4u *)(kv + (i + 4) * 2048 + 1024);
        const f32x4 d = s[i] - m;
        v2u p;
        p.x = pk2(__builtin_amdgcn_exp2f(d[0]), __builtin_amdgcn_exp2f(d[1]));
        p.y = pk2(__builtin_amdgcn_exp2f(d[2]), __builtin_amdgcn_exp2f(d[3]));
        o0 = mfma16(v2u{vf[i].x, vf[i].y}, p, o0);
        o1 = mfma16(v2u{vf[i].z, vf[i].w}, p, o1);
        if(i < 8 && i * 8 + w < W1_EARLY) dma1k(rws, lane * 16, WT(ID_W1 + slot * 98 + i * 8 + w), ldsb + L_X + (i * 8 + w) * 1024);
        if(layer == 0 && i >= 8 && i < 16) W2LOAD(i - 8);
        if(i & 1) SB();
      }
      const float inv = __builtin_amdgcn_rcpf(__shfl(o1[0], 48 + li));
      o0 *= inv, o1 *= inv;
      const v4u of = {pk2(o0[0], o0[1]), pk2(o0[2], o0[3]), pk2(o1[0], o1[1]), pk2(o1[2], o1[3])};
      st16(of, rws, lane * 16, PB(t, layer) + FO(F_O + w * 16 + slot));
      DRAIN();
      BARRIER();
      if(w == 7 && lane == 0) set_word(ws, FLAG(t, layer, FL_O, slot), tag);
    }
    if(SLOT_OF(b0) < 14) {
      IDS();
      if(w == 7) wait_flags(ws, FLAG(t, layer, FL_O, li), tag);
      const rsrc_t rx = layer == 0 ? __builtin_amdgcn_make_buffer_rsrc((void *)x, 0, S * D * 2, 0x00020000) : rws;
      const v4u xr = ld16(rx, layer == 0 ? (16 * tt + li) * (D * 2) + g * 16 : lane * 16,
          layer == 0 ? slot * 64 : PB(t, 2) + FO(F_X2 + w * 16 + slot));
      BARRIER();
      v4u of[16];
      const int ob = opq(PB(t, layer) + FO(F_O + w * 16));
#pragma unroll
      for(int h = 0; h < 8; ++h) of[h] = ld16(rws, lane * 16 + (h & 3) * 1024, ob + X4(h));
      SB();
      v4u wf[32];
#pragma unroll
      for(int i = 0; i < 8; ++i) wf[i] = *(LAS v4u *)(L + L_WO + i * 1024 + lane * 16);
      SB();
      f32x4 a0 = {0.f, 0.f, 0.f, 0.f}, a1 = {0.f, 0.f, 0.f, 0.f};
#pragma unroll
      for(int h = 0; h < 16; ++h) {
        if(h < 8) of[h + 8] = ld16(rws, lane * 16 + (h & 3) * 1024, ob + X4(h + 8));
        if(h < 12)
          wf[2 * h + 8] = *(LAS v4u *)(L + L_WO + (2 * h + 8) * 1024 + lane * 16),
                     wf[2 * h + 9] = *(LAS v4u *)(L + L_WO + (2 * h + 9) * 1024 + lane * 16);
        a0 = mfma32(wf[2 * h], of[h], a0), a1 = mfma32(wf[2 * h + 1], of[h], a1);
        SB();
      }
      const v4u y = {pk2(a0[0] + bflo(xr.x), a0[1] + bfhi(xr.x)), pk2(a0[2] + bflo(xr.y), a0[3] + bfhi(xr.y)),
          pk2(a1[0] + bflo(xr.z), a1[1] + bfhi(xr.z)), pk2(a1[2] + bflo(xr.w), a1[3] + bfhi(xr.w))};
      st16(y, rws, lane * 16, PB(t, layer) + FO(F_X1 + w * 16 + slot));
      st8(piece_stats(y), rws, lane < 16 ? lane * 8 : OOB, PB(t, 2) + FO(F_ST + (layer * 8 + w) * 2) + slot * 128);
      DRAIN();
      BARRIER();
      if(w == 7 && lane == 0) set_word(ws, FLAG(t, layer, FL_X1, slot), tag);
    }
    {
      IDS();
      const int xb = opq(PB(t, layer) + FO(F_X1 + w * 16));
      if(w == 7) wait_flags(ws, FLAG(t, layer, FL_X1, (li < 14 ? li : 0)), tag);
      if(layer == 0 && ti < 112) {
        const int nt = ti >> 4, i = ti & 15;
        float sum = 0.f;
#pragma unroll
        for(int ks = 0; ks < 14; ++ks)
          sum += __uint_as_float(__builtin_amdgcn_raw_buffer_load_b32(rws, OFF_CSP + (ID_W1 + slot * 98 + ks * 7) * 64 + ti * 4, 0, AUX_SC1));
        *(LAS float *)(L + LC_CS1 + ti * 4) = sum;
        const int col = nt < 6 ? 112 * slot + 32 * (nt >> 1) + 8 * (i >> 2) + (i & 3) + 4 * (nt & 1) : 112 * slot + 96 + i;
        *(LAS float *)(L + LC_B1 + ti * 4) = bflo(b1[col]);
      }
      BARRIER();
      v4u xf[14];
      v2u sp[4];
      ROW_STATS(layer);
#pragma unroll
      for(int ks = 0; ks < XRING; ++ks) XLOAD(ks);
      SB();
      WT_DMA(ID_W1 + slot * 98, W1_EARLY, 98, w, 8, L_X);
      f32x4 acc[7];
#pragma unroll
      for(int n = 0; n < 7; ++n) acc[n] = f32x4{0.f, 0.f, 0.f, 0.f};
      float rs, nmr;
      const int wb = opq(ID_W2 + CSL(slot) * 224 + w * 28);
      SB();
#define STEP_B(ks) \
  if((ks) == 3) {   \
    SUM_STATS(); \
    const float mu = s1 * (1.f / D), var = fmaxf(s2 * (1.f / D) - mu * mu, 0.f), r1 = __builtin_amdgcn_rsqf(var + EPS); \
    rs = r1 * __builtin_amdgcn_rsqf(var * r1 * r1 + EPS), nmr = -mu * rs; \
    asm volatile("" : "+v"(rs), "+v"(nmr)); \
  } \
  STEP_X(ks); \
  if(layer == 0 && (ks) >= 6) { \
    _Pragma("unroll") for(int j = 8 + ((ks)-6) * 3; j < 8 + ((ks)-5) * 3 && j < 28; ++j) W2LOAD(j); \
  }
      GEMM_PART(7, 0, 7, 0, 9, 7, STEP_B);
      asm volatile("s_waitcnt vmcnt(%0)" ::"i"(14 - XRING) : "memory");
      BARRIER();
      GEMM_PART(7, 0, 7, 9, 14, 7, STEP_B);
      const int ho = PB(t, 0) + FO(F_H + w * 64 + (slot >> 1) * 8);
      f32x4 cc[7], bb[7];
#pragma unroll
      for(int n = 0; n < 7; ++n) cc[n] = *(LAS f32x4 *)(L + LC_CS1 + n * 64 + g * 16), bb[n] = *(LAS f32x4 *)(L + LC_B1 + n * 64 + g * 16);
      SB();
      unsigned hp[14];
#pragma unroll
      for(int n = 0; n < 7; ++n) {
        const f32x4 v = acc[n] * rs + (cc[n] * nmr + bb[n]);
        const f32x4 a = v * (v * v * -0.1029432396f + -2.3022081986f);
        const f32x4 e = {__builtin_amdgcn_exp2f(a[0]), __builtin_amdgcn_exp2f(a[1]), __builtin_amdgcn_exp2f(a[2]), __builtin_amdgcn_exp2f(a[3])};
        const f32x4 d = e + 1.f;
        const f32x4 r = {__builtin_amdgcn_rcpf(d[0]), __builtin_amdgcn_rcpf(d[1]), __builtin_amdgcn_rcpf(d[2]), __builtin_amdgcn_rcpf(d[3])};
        const f32x4 hv = v * r;
        hp[2 * n] = pk2(hv[0], hv[1]), hp[2 * n + 1] = pk2(hv[2], hv[3]);
        if(n & 1)
          st16(v4u{hp[2 * n - 2], hp[2 * n - 1], hp[2 * n], hp[2 * n + 1]}, rws, lane * 16,
              ho + (n < 4 ? 2 * (slot & 1) + (n >> 1) : 4 + (slot & 1)) * 1024);
        if(n == 6) st8(v2u{hp[12], hp[13]}, rws, lane * 16, ho + 6144 + (slot & 1) * 8);
      }
      DRAIN();
      BARRIER();
      if(w == 7) {
        if(lane == 0) set_word(ws, FLAG(t, layer, FL_H, slot), tag);
        if(slot < 14) wait_flags(ws, FLAG(t, layer, FL_H, li), tag);
      } else if(layer == 0 && slot < 14) {
        WT_DMA(ID_WQ + slot * 84, 0, WQ_HOP, w, 7, L_X);
      }
    }
    if(SLOT_OF(b0) < 14) {
      IDS();
      const int csl = CSL(slot), chalf = slot >= 7;
      BARRIER();
      const int ho = opq(PB(t, 0) + FO(F_H + 4 * chalf * 64 + w * 8));
      v4u hf[8];
#pragma unroll
      for(int i = 0; i < 8; ++i) hf[i] = ld16(rws, lane * 16 + (i >> 2) * 1024, ho + (i & 3) * FO(64));
      const int ow = 4 * chalf + (w >> 1), oks = 2 * csl + (w & 1);
      v4u xr, bb;
      SB();
      f32x4 acc[4][4];
#pragma unroll
      for(int q = 0; q < 4; ++q)
#pragma unroll
        for(int n = 0; n < 4; ++n) acc[q][n] = f32x4{0.f, 0.f, 0.f, 0.f};
#pragma unroll
      for(int k = 0; k < 7; ++k) {
#pragma unroll
        for(int q = 0; q < 4; ++q) {
          const v4u cur = hf[(k & 1) * 4 + q];
          if(k + 2 < 7) hf[(k & 1) * 4 + q] = ld16(rws, lane * 16 + (k & 3) * 1024, ho + q * FO(64) + 2048 + (k >> 2) * 4096);
          if(k == 5 && q == 1) xr = ld16(rws, lane * 16, opq(PB(t, layer) + FO(F_X1 + ow * 16 + oks)));
          if(k == 5 && q == 2)
            bb = __builtin_amdgcn_raw_buffer_load_b128(__builtin_amdgcn_make_buffer_rsrc((void *)b2, 0, D * 2, 0x00020000), g * 16, opq(oks * 64), 0);
#pragma unroll
          for(int n = 0; n < 4; ++n) acc[q][n] = mfma32(w2f[k * 4 + n], cur, acc[q][n]);
          SB();
        }
      }
      if(layer == 0) WT_DMA(ID_WQ + slot * 84, WQ_HOP, 84, w, 8, L_X);
      f32x4 a0, a1;
#pragma unroll
      for(int f = 0; f < 8; ++f) {
        const f32x4 p0 = acc[f >> 1][2 * (f & 1)], p1 = acc[f >> 1][2 * (f & 1) + 1];
        if(f == w) a0 = p0, a1 = p1;
        else
          *(LAS v4u *)(L + L_RED + (f * 7 + (w < f ? w : w - 1)) * 1024 + lane * 16) =
              v4u{pk2(p0[0], p0[1]), pk2(p0[2], p0[3]), pk2(p1[0], p1[1]), pk2(p1[2], p1[3])};
      }
      BARRIER();
      {
        const unsigned one = g == (li >> 2) ? ((li & 1) ? 0x3F800000u : 0x00003F80u) : 0u;
        const v4u e0 = {(li & 2) ? 0u : one, (li & 2) ? one : 0u, 0u, 0u}, e1 = {0u, 0u, (li & 2) ? 0u : one, (li & 2) ? one : 0u};
        v4u p[7];
#pragma unroll
        for(int s = 0; s < 7; ++s) p[s] = *(LAS v4u *)(L + L_RED + (w * 7 + s) * 1024 + lane * 16);
        a0 = mfma32(e0, xr, a0), a1 = mfma32(e1, xr, a1);
        a0 = mfma32(e0, bb, a0), a1 = mfma32(e1, bb, a1);
#pragma unroll
        for(int s = 0; s < 7; ++s) a0 = mfma32(e0, p[s], a0), a1 = mfma32(e1, p[s], a1);
      }
      const v4u y = {pk2(a0[0], a0[1]), pk2(a0[2], a0[3]), pk2(a1[0], a1[1]), pk2(a1[2], a1[3])};
      if(layer == 0) {
        st16(y, rws, lane * 16, PB(t, 2) + FO(F_X2 + ow * 16 + oks));
        st8(piece_stats(y), rws, lane < 16 ? lane * 8 : OOB, PB(t, 2) + FO(F_ST + (16 + ow) * 2) + oks * 128);
      } else {
        *(GAS v4u *)(out + (16 * (8 * t + ow) + li) * D + 32 * oks + 8 * g) = y;
      }
    }
    if(layer == 0) {
      IDS();
      if(slot >= 14) WT_DMA(ID_WQ + slot * 84, 0, 84, w, 8, L_X);
      DRAIN();
      BARRIER();
      if(w == 7) {
        if(lane == 0 && slot < 14) set_word(ws, FLAG(t, 0, FL_X2, slot), tag);
        wait_flags(ws, FLAG(t, 0, FL_X2, (li < 14 ? li : 0)), tag);
      }
      BARRIER();
    }
  };
  run_layer(IntC<0>{});
  run_layer(IntC<1>{});
  asm volatile(".Lfk_end:");
}
}

extern "C" void kernel_launch(void *const *d_in, const int *in_sizes, int n_in, void *d_out, int out_size, void *d_ws, size_t ws_size,
    hipStream_t stream) {
  const size_t pad = (size_t)(-(intptr_t)d_ws) & (size_t)0x1FFFFF;
  fused_kernel<<<NWG, NTHR, 0, stream>>>((const u16 *)d_in[0], (const u16 *)d_in[1], (const u16 *)d_in[2], (const u16 *)d_in[4],
      (const u16 *)d_in[5], (const u16 *)d_in[6], (const u16 *)d_in[7], (u16 *)d_out, (unsigned char *)d_ws + pad);
}
```

```cpp
#include <hip/hip_runtime.h>
#include <stdint.h>

#define GAS __attribute__((address_space(1)))
#define LAS __attribute__((address_space(3)))
#define DI __device__ __forceinline__
typedef unsigned v4u __attribute__((ext_vector_type(4)));
typedef unsigned v2u __attribute__((ext_vector_type(2)));
typedef float f32x4 __attribute__((ext_vector_type(4)));
typedef float f32x2 __attribute__((ext_vector_type(2)));
typedef __bf16 bf16x8 __attribute__((ext_vector_type(8)));
typedef __bf16 bf16x2 __attribute__((ext_vector_type(2)));
typedef short s16x4 __attribute__((ext_vector_type(4)));
typedef unsigned long long u64;
typedef unsigned short u16;
typedef __amdgpu_buffer_rsrc_t rsrc_t;

#define TILE_OF(b) ((((b)&7) >> 1) * 4 + ((b) >> 6))
#define SLOT_OF(b) (((b)&1) * 8 + (((b) >> 3) & 7))
#define CS14(s) ((s) >= 14 ? 0 : (s))
#define CSL(s) ((s) >= 14 ? 0 : (s) >= 7 ? (s)-7 : (s))

namespace {
template <int V> struct IntC {
  static constexpr int value = V;
};
constexpr int S = 2048, D = 448, F = 1792;
constexpr int NWG = 256, NTHR = 512;
constexpr float EPS = 1e-5f;
constexpr int AUX_SC1 = 16;
constexpr unsigned SPIN_CAP = 1u << 18;
constexpr int OOB = 0x7F000000;
constexpr int XRING = 8;
constexpr int WDEP = 7;
constexpr u64 VOTE = 1ull << 63;

constexpr int FS = 128;
constexpr int OFF_EPOCH = 0, OFF_PREPG = 4096, OFF_PREPF = 8192;
constexpr int OFF_CSP = 1 << 20;
constexpr int OFF_WT = 2 << 20;
constexpr int ID_WO = 0, ID_W1 = 448, ID_W2 = 2016, ID_WQ = 3584, ID_END = 4928;
constexpr int OFF_PROBE = 12 << 20;
constexpr int PG0 = 8, WS_BYTES = (PG0 + 48) << 21;
constexpr int F_KV = 0, F_O = 256, F_X1 = 384, F_H = 512, F_X2 = 0, F_ST = 128, F_FL = 192;
constexpr int FL_KV = 0, FL_O = 1, FL_X1 = 2, FL_H = 3, FL_X2 = 4;
#define FO(f) ((((f) >> 3) << 14) + (((f)&7) << 10))
#define PB(t_, p_) (((PG0 + 3 * (t_) + (p_)) << 21) + (((side ^ (t_) ^ (p_) ^ ((t_) >> 3)) & 1) << 13))
#define FLAG(t_, layer_, ph_, i_) (PB(t_, 2) + FO(F_FL + ((layer_)*5 + (ph_)) * 2) + (i_)*FS)
#define WT(f) (OFF_WT + (f)*1024)
static_assert(OFF_CSP + ID_END * 128 <= OFF_WT && OFF_WT + ID_END * 1024 <= OFF_PROBE && (PG0 & 1) == 0, "map");

constexpr int L_X = 0;
constexpr int L_KV = 100352 - 32768;
constexpr int L_WO = 100352 + 16384;
constexpr int L_RED = 86016;
constexpr int L_C = L_WO + 32768;
constexpr int LC_CSQ = L_C, LC_CS1 = L_C + 512, LC_B1 = L_C + 1024;
constexpr int LS_Q = L_C + 1536, LS_HOLD = LS_Q + 4, LS_KV = LS_Q + 8, LS_SIDE = LS_Q + 32;
constexpr int LDS_BYTES = L_C + 2048;
constexpr int W1_EARLY = 63;
constexpr int WQ_HOP = 56;
static_assert(W1_EARLY * 1024 <= L_KV && L_RED + 56 * 1024 <= L_C, "LDS map");

DI f32x4 mfma32(v4u a, v4u b, f32x4 c) {
  return __builtin_amdgcn_mfma_f32_16x16x32_bf16(__builtin_bit_cast(bf16x8, a), __builtin_bit_cast(bf16x8, b), c, 0, 0, 0);
}
DI f32x4 mfma16(v2u a, v2u b, f32x4 c) {
  return __builtin_amdgcn_mfma_f32_16x16x16bf16_1k(__builtin_bit_cast(s16x4, a), __builtin_bit_cast(s16x4, b), c, 0, 0, 0);
}
DI unsigned pk2(float a, float b) {
  f32x2 v = {a, b};
  return __builtin_bit_cast(unsigned, __builtin_convertvector(v, bf16x2));
}
DI float bflo(unsigned u) { return __uint_as_float(u << 16); }
DI float bfhi(unsigned u) { return __uint_as_float(u & 0xFFFF0000u); }
DI float dot2(unsigned a, unsigned b, float c) {
  return __builtin_amdgcn_fdot2_f32_bf16(__builtin_bit_cast(bf16x2, a), __builtin_bit_cast(bf16x2, b), c, false);
}
DI float gsum(float x) {
  auto r = __builtin_amdgcn_permlane32_swap(__float_as_uint(x), __float_as_uint(x), false, false);
  x = __uint_as_float(r[0]) + __uint_as_float(r[1]);
  auto q = __builtin_amdgcn_permlane16_swap(__float_as_uint(x), __float_as_uint(x), false, false);
  return __uint_as_float(q[0]) + __uint_as_float(q[1]);
}
DI float gmax(float x) {
  auto r = __builtin_amdgcn_permlane32_swap(__float_as_uint(x), __float_as_uint(x), false, false);
  x = fmaxf(fmaxf(x, __uint_as_float(r[0])), __uint_as_float(r[1]));
  auto q = __builtin_amdgcn_permlane16_swap(__float_as_uint(x), __float_as_uint(x), false, false);
  return fmaxf(fmaxf(x, __uint_as_float(q[0])), __uint_as_float(q[1]));
}
DI v4u ld16(rsrc_t rs, int voff, int soff) { return __builtin_amdgcn_raw_buffer_load_b128(rs, voff, soff, AUX_SC1); }
DI v2u ld8(rsrc_t rs, int voff, int soff) { return __builtin_amdgcn_raw_buffer_load_b64(rs, voff, soff, AUX_SC1); }
DI void st16(v4u v, rsrc_t rs, int voff, int soff) {
  __builtin_amdgcn_raw_buffer_store_b128(v, rs, voff, soff, AUX_SC1);
  asm volatile("s_nop 1" ::"v"(v) : "memory");
}
DI void st8(v2u v, rsrc_t rs, int voff, int soff) { __builtin_amdgcn_raw_buffer_store_b64(v, rs, voff, soff, AUX_SC1); }
DI int opq(int v) {
  asm volatile("" : "+s"(v));
  return v;
}
DI int opqv(int v) {
  asm volatile("" : "+v"(v));
  return v;
}
DI v2u trread(LAS unsigned char *p) { return __builtin_bit_cast(v2u, __builtin_amdgcn_ds_read_tr16_b64_v4i16((LAS s16x4 *)p)); }
#define DRAIN() asm volatile("s_waitcnt vmcnt(0)" ::: "memory")
#define BARRIER() asm volatile("s_waitcnt lgkmcnt(0)\n\ts_barrier" ::: "memory")
#define SB() __builtin_amdgcn_sched_barrier(0)
#define dma1k(rs, voff, soff, ldsaddr) \
  asm volatile("s_mov_b32 m0, %0\n\ts_nop 0\n\tbuffer_load_dwordx4 %1, %2, %3 offen sc1 lds" ::"s"((unsigned)(ldsaddr)), "v"((int)(voff)), \
               "s"(rs), "s"((int)(soff)) \
               : "memory")
#define dma1k_input(rs, voff, soff, ldsaddr) \
  asm volatile("s_mov_b32 m0, %0\n\ts_nop 0\n\tbuffer_load_dwordx4 %1, %2, %3 offen lds" ::"s"((unsigned)(ldsaddr)), "v"((int)(voff)), \
               "s"(rs), "s"((int)(soff)) \
               : "memory")
DI void long_wait(unsigned spin) {
  if((spin & 1023u) == 1023u) __builtin_amdgcn_fence(__ATOMIC_ACQUIRE, "agent");
}
DI void wait_flags(const unsigned char *ws, int off, u64 tag) {
  const GAS u64 *p = (const GAS u64 *)(ws + off);
  for(unsigned spin = 0; spin < SPIN_CAP; ++spin) {
    const u64 v = __hip_atomic_load(p, __ATOMIC_RELAXED, __HIP_MEMORY_SCOPE_AGENT);
    if(__all(v == tag)) break;
    __builtin_amdgcn_s_sleep(1);
    long_wait(spin);
  }
  __builtin_amdgcn_fence(__ATOMIC_ACQUIRE, "wavefront");
}
DI void set_word(unsigned char *ws, int off, u64 v) {
  __hip_atomic_store((GAS u64 *)(ws + off), v, __ATOMIC_RELAXED, __HIP_MEMORY_SCOPE_AGENT);
}
DI void set_flag(unsigned char *ws, int off, int lane, u64 v) {
  if(lane < 16) set_word(ws, off + lane * 8, v);
}
DI unsigned lds_count(LAS unsigned char *p, unsigned add, int lane) {
  unsigned old = 0;
  if(lane == 0) old = __hip_atomic_fetch_add((LAS unsigned *)p, add, __ATOMIC_RELAXED, __HIP_MEMORY_SCOPE_WORKGROUP);
  return __builtin_amdgcn_readfirstlane(old);
}
DI v2u piece_stats(v4u y) {
  float s1 = 0.f, s2 = 0.f;
#pragma unroll
  for(int j = 0; j < 4; ++j) s1 = dot2(y[j], 0x3F803F80u, s1), s2 = dot2(y[j], y[j], s2);
  return v2u{__float_as_uint(gsum(s1)), __float_as_uint(gsum(s2))};
}

__global__ void __launch_bounds__(NTHR, 2)
    fused_kernel(const u16 *__restrict__ x, const u16 *__restrict__ wqkv, const u16 *__restrict__ wo, const u16 *__restrict__ w1,
        const u16 *__restrict__ b1, const u16 *__restrict__ w2, const u16 *__restrict__ b2, u16 *__restrict__ out, unsigned char *ws) {
  __shared__ __attribute__((aligned(16))) unsigned char smem[LDS_BYTES];
  LAS unsigned char *L = (LAS unsigned char *)smem;
  const unsigned ldsb = (unsigned)(size_t)L;
  const int tid = threadIdx.x, w0 = __builtin_amdgcn_readfirstlane(tid >> 6), b0 = blockIdx.x;
#define IDS() \
  const int ti = opqv(tid), lane = ti & 63, w = opq(w0), b = opq(b0), t = TILE_OF(b), slot = SLOT_OF(b); \
  const int tt = 8 * t + w, g = lane >> 4, li = lane & 15; \
  (void)g, (void)li, (void)tt, (void)slot
#define WT_DMA(id0, from, to, w_, nw_, ldsoff) \
  for(int i_ = (from) + (w_); i_ < (to); i_ += (nw_)) dma1k(rws, lane * 16, WT((id0) + i_), ldsb + (ldsoff) + i_ * 1024)
#define GEMM_PART(NT, N0, NN, K0, K1, SWAPN, STEP) \
  { \
    constexpr int CNT = ((K1) - (K0)) * (NN); \
    v4u wr[WDEP]; \
    _Pragma("unroll") for(int i = 0; i < WDEP; ++i) wr[i] = *(LAS v4u *)(L + L_X + (((K0) + i / (NN)) * (NT) + (N0) + i % (NN)) * 1024 + lane * 16); \
    SB(); \
    _Pragma("unroll") for(int i = 0; i < CNT; ++i) { \
      const int ks = (K0) + i / (NN), n = (N0) + i % (NN); \
      if(i % (NN) == 0) { \
        STEP(ks); \
      } \
      const v4u cur = wr[i % WDEP]; \
      if(i + WDEP < CNT) \
        wr[i % WDEP] = *(LAS v4u *)(L + L_X + (((K0) + (i + WDEP) / (NN)) * (NT) + (N0) + (i + WDEP) % (NN)) * 1024 + lane * 16); \
      acc[n] = n < (SWAPN) ? mfma32(cur, xf[ks], acc[n]) : mfma32(xf[ks], cur, acc[n]); \
      SB(); \
    } \
  }
#define X4(k) ((((k) >> 3) << 14) + ((((k) >> 2) & 1) << 12))
#define XLOAD(k) xf[k] = ld16(rws, lane * 16 + ((k)&3) * 1024, xb + X4(k))
#define STEP_NONE(ks)
#define STEP_X(ks) \
  if((ks) + XRING < 14) XLOAD((ks) + XRING)
#define ROW_STATS(which) \
  _Pragma("unroll") for(int i = 0; i < 4; ++i) \
      sp[i] = ld8(rws, (i == 3 && g > 1) ? OOB : li * 8 + g * 128 + i * 512, PB(t, 2) + FO(F_ST + ((which)*8 + w) * 2))
#define SUM_STATS() \
  const float s1 = gsum((__uint_as_float(sp[0].x) + __uint_as_float(sp[1].x)) + (__uint_as_float(sp[2].x) + __uint_as_float(sp[3].x))); \
  const float s2 = gsum((__uint_as_float(sp[0].y) + __uint_as_float(sp[1].y)) + (__uint_as_float(sp[2].y) + __uint_as_float(sp[3].y)))
#define W2LOAD(j) w2f[j] = ld16(rws, lane * 16 + ((j)&3) * 1024, WT(wb + ((j) & ~3)))
  const rsrc_t rws = __builtin_amdgcn_make_buffer_rsrc((void *)ws, 0, WS_BYTES, 0x00020000);
  u64 tag = 0;
  int side = 0;
  {
    u64 pc;
    int len;
    asm volatile("s_getpc_b64 %0\n.Lfk_here:\n\ts_mov_b32 %1, .Lfk_end-.Lfk_here" : "=s"(pc), "=s"(len));
    if(w0 == 7) {
      const rsrc_t rc = __builtin_amdgcn_make_buffer_rsrc((void *)pc, 0, len, 0x00020000);
      asm volatile("s_nop 4" ::: "memory");
      dma1k_input(rc, (tid & 63) * 16, (b0 >> 3) * 2048, ldsb + L_WO + 30720);
      dma1k_input(rc, (tid & 63) * 16, (b0 >> 3) * 2048 + 1024, ldsb + L_WO + 31744);
    }
    if(w0 == 6)
      asm volatile("s_mov_b32 m0, %0\n\ts_nop 0\n\tbuffer_load_dword %1, %2, 0 offen sc1 lds" ::"s"(ldsb + L_WO + 28672),
                   "v"((tid & 63) < 9 ? (max(PG0 + 3 * (TILE_OF(b0) - 2) + (tid & 63), 0) << 21) + FO(1023) + (b0 & 7) * 128 : OOB),
                   "s"(rws)
                   : "memory");
  }
  v4u w2f[28];

  auto run_layer = [&](auto LC) __attribute__((always_inline)) {
    constexpr int layer = decltype(LC)::value;
    v4u qf;
    {
      IDS();
      v4u xf[14];
      float mu, rs, mur[4], rsr[4];
      f32x4 acc[6];
#pragma unroll
      for(int n = 0; n < 6; ++n) acc[n] = f32x4{0.f, 0.f, 0.f, 0.f};
#define A_FINISH_STATS() \
  mu = s1 * (1.f / D), rs = __builtin_amdgcn_rsqf(fmaxf(s2 * (1.f / D) - mu * mu, 0.f) + EPS); \
  _Pragma("unroll") for(int r = 0; r < 4; ++r) mur[r] = __shfl(mu, 4 * g + r), rsr[r] = __shfl(rs, 4 * g + r); \
  asm volatile("" : "+v"(mu), "+v"(rs), "+v"(mur[0]), "+v"(mur[1]), "+v"(mur[2]), "+v"(mur[3]), "+v"(rsr[0]), "+v"(rsr[1]), "+v"(rsr[2]), "+v"(rsr[3]))
      if(layer == 0) {
        int dst[2], dst1[2], tro[2], tre[2];
        unsigned char *pp = ws + OFF_PROBE + (b & 31) * 16384 + w * 512;
        if(w < 3 && b >= 224) {
          asm volatile("global_store_dwordx2 %0, %1, off sc1" ::"v"(pp + (lane >> 5) * 8192 + (lane & 31) * 8), "v"(0ull) : "memory");
        } else if(w < 3) {
          const int r = lane >> 2, c = lane & 3, head = b / 14, ks = b - 14 * head;
          const rsrc_t rq = __builtin_amdgcn_make_buffer_rsrc((void *)wqkv, 0, D * 3 * D * 2, 0x00020000);
          const int o0 = (32 * ks + r) * 2688 + w * 896 + 56 * head + c * 16;
          dma1k_input(rq, o0, 0, ldsb + L_WO + w * 2048);
          dma1k_input(rq, o0, 16 * 2688, ldsb + L_WO + w * 2048 + 1024);
          dst[0] = ID_WQ + head * 84 + ks * 6 + w * 2, dst1[0] = dst[0] + 1, tro[0] = 16, tre[0] = 8;
        }
        const v2u ep = ld8(rws, OFF_EPOCH, 0);
        if(ti < 8) *(LAS unsigned *)(L + LS_Q + 4 * ti) = 0u;
        const rsrc_t rx = __builtin_amdgcn_make_buffer_rsrc((void *)x, 0, S * D * 2, 0x00020000);
        const int xvo = (16 * tt + (lane >> 3)) * (D * 2) + (((lane & 7) ^ (lane >> 3)) * 16);
#define X0_DMA() \
  _Pragma("unroll") for(int i0 = 0; i0 < 14; ++i0) { \
    const int i = i0 + 2 * (slot & 7) >= 14 ? i0 + 2 * (slot & 7) - 14 : i0 + 2 * (slot & 7); \
    dma1k_input(rx, xvo, (i & 1) * 8 * (D * 2) + (i >> 1) * 128, ldsb + w * 14336 + i * 1024); \
  }
#define TAKE_TAG() \
  DRAIN(); \
  tag = (((u64)(unsigned)__builtin_amdgcn_readfirstlane(ep.y) << 32) | (unsigned)__builtin_amdgcn_readfirstlane(ep.x)) + 1ull
        v4u pf0[2], pf1[2];
        unsigned pcs[2];
#define PREP_READ(rd, QPAD) \
  { \
    LAS unsigned char *p = L + L_WO + ((rd)*8 + w) * 2048 + (8 * g + (li >> 2)) * 64 + (li & 3) * tro[rd]; \
    const v2u a0 = trread(p), a1 = trread(p + 256), c0 = trread(p + tre[rd]), c1 = trread(p + tre[rd] + 256); \
    pf0[rd] = v4u{a0.x, a0.y, a1.x, a1.y}, pf1[rd] = v4u{c0.x, c0.y, c1.x, c1.y}; \
    if(QPAD && li >= 12) pf1[rd] = v4u{0u, 0u, 0u, 0u}; \
    float ca = 0.f, cb = 0.f; \
    _Pragma("unroll") for(int j = 0; j < 4; ++j) ca = dot2(pf0[rd][j], 0x3F803F80u, ca), cb = dot2(pf1[rd][j], 0x3F803F80u, cb); \
    ca = gsum(ca), cb = gsum(cb); \
    pcs[rd] = __float_as_uint(g > 1 ? cb : ca); \
    asm volatile("" : "+v"(pf0[rd]), "+v"(pf1[rd]), "+v"(pcs[rd])); \
  }
#define PREP_WRITE(rd) \
  if(dst[rd] < ID_END) { \
    st16(pf0[rd], rws, lane * 16, WT(dst[rd])); \
    st16(pf1[rd], rws, lane * 16, WT(dst1[rd])); \
    if(dst[rd] >= ID_W1 && (dst[rd] < ID_W2 || dst[rd] >= ID_WQ)) \
      __builtin_amdgcn_raw_buffer_store_b32(pcs[rd], rws, OFF_CSP + (g > 1 ? dst1[rd] : dst[rd]) * 128 + (lane & 31) * 4, 0, AUX_SC1); \
  }
        BARRIER();
        if(w < 3) {
          unsigned vote = 1u;
          TAKE_TAG();
          if(b >= 224) {
            unsigned v;
            unsigned best[2] = {~0u, ~0u};
#pragma unroll
            for(int i = 0; i < 4; ++i) {
              u64 t0, t1;
              asm volatile(
                  "s_memtime %0\n\ts_waitcnt lgkmcnt(0)\n\tglobal_load_dword %2, %3, off sc1\n\ts_waitcnt vmcnt(0)\n\ts_memtime %1\n\ts_waitcnt lgkmcnt(0)"
                  : "=&s"(t0), "=&s"(t1), "=&v"(v)
                  : "v"(pp + ((i ^ w ^ (b >> 3)) & 1) * 8192 + (i >> 1) * 128)
                  : "memory");
              const unsigned dt = (unsigned)(t1 - t0);
              if((i ^ w ^ (b >> 3)) & 1) best[1] = min(best[1], dt);
              else best[0] = min(best[0], dt);
            }
            if((best[1] < best[0]) != (bool)((b >> 2) & 1)) vote = 0x101u;
          } else {
            PREP_READ(0, true);
            PREP_WRITE(0);
          }
          lds_count(L + LS_HOLD, 1u, lane);
          DRAIN();
          const unsigned cnt = lds_count(L + LS_Q, vote, lane) + vote;
          if((cnt & 0xFFu) == 3u) set_flag(ws, OFF_PREPF + b * FS, lane, cnt >= 0x200u ? tag ^ VOTE : tag);
          X0_DMA();
          DRAIN();
        } else {
#pragma unroll
          for(int rd = 0; rd < 2; ++rd) {
            const int id = rd == 0 ? b * 5 + (w - 3) : w < 5 ? 1280 + b * 2 + (w - 3) : 1792, r = lane >> 2, c = lane & 3;
            const u16 *src;
            int bytes, o0, o1;
            tro[rd] = 16, tre[rd] = 8;
            if(id < 224) {
              const int ks = id / 14, sl = id - ks * 14;
              src = wo, bytes = D * D * 2;
              o0 = (28 * ks + r) * 896 + 64 * sl + c * 16, o1 = r < 12 ? o0 + 16 * 896 : OOB;
              dst[rd] = ID_WO + sl * 32 + ks * 2, dst1[rd] = dst[rd] + 1;
            } else if(id < 224 + 672) {
              const int i2 = id - 224, ks = i2 / 48, rem = i2 - ks * 48, sl = rem / 3, p = rem - sl * 3;
              src = w1, bytes = D * F * 2;
              o0 = (32 * ks + r) * 3584 + 224 * sl + 64 * p + c * 16, o1 = o0 + 16 * 3584;
              dst[rd] = ID_W1 + sl * 98 + ks * 7 + 2 * p, dst1[rd] = dst[rd] + 1;
            } else if(id < 896 + 112) {
              const int i2 = id - 896, ks = i2 >> 3, sl = 2 * (i2 & 7);
              src = w1, bytes = D * F * 2;
              o0 = (32 * ks + r) * 3584 + 224 * (sl + (c >> 1)) + 192 + (c & 1) * 16, o1 = o0 + 16 * 3584;
              dst[rd] = ID_W1 + sl * 98 + ks * 7 + 6, dst1[rd] = dst[rd] + 98, tro[rd] = 8, tre[rd] = 32;
            } else {
              const int i2 = id - 1008, hks = i2 / 14, cb = i2 - hks * 14, wv = hks / 7, kk = hks - 7 * wv;
              src = w2, bytes = F * D * 2;
              const int row = 224 * wv +
                  (kk < 4 ? 112 * (kk >> 1) + 32 * (kk & 1) + r : kk < 6 ? 112 * (kk - 4) + 64 + r : 96 + 112 * ((r >> 2) & 1) + 4 * (r >> 3) + (r & 3));
              o0 = row * 896 + 64 * cb + c * 16, o1 = o0 + (kk < 6 ? 16 : 8) * 896;
              dst[rd] = ID_W2 + (cb >> 1) * 224 + hks * 4 + (cb & 1) * 2, dst1[rd] = dst[rd] + 1;
              if(id >= 1792) o0 = o1 = OOB, dst[rd] = dst1[rd] = ID_END;
            }
            const rsrc_t rsw = __builtin_amdgcn_make_buffer_rsrc((void *)src, 0, bytes, 0x00020000);
            asm volatile("s_nop 4" ::: "memory");
            dma1k_input(rsw, o0, 0, ldsb + L_WO + (rd * 8 + w) * 2048);
            dma1k_input(rsw, o1, 0, ldsb + L_WO + (rd * 8 + w) * 2048 + 1024);
          }
          for(unsigned spin = 0; spin < (1u << 20); ++spin)
            if(__builtin_amdgcn_readfirstlane(__hip_atomic_load((LAS unsigned *)(L + LS_HOLD), __ATOMIC_RELAXED, __HIP_MEMORY_SCOPE_WORKGROUP)) >= 3)
              break;
          X0_DMA();
          TAKE_TAG();
          if(w == 7) wait_flags(ws, OFF_PREPF + (slot * 14 + (li < 14 ? li : 0)) * FS, tag);
        }
#pragma unroll
        for(int ks = 0; ks < 14; ++ks)
          xf[ks] = *(LAS v4u *)(L + w * 14336 + ((ks >> 1) * 2 + (li >> 3)) * 1024 + (li & 7) * 128 + (((ks & 1) * 4 + g) ^ (li & 7)) * 16);
        BARRIER();
        WT_DMA(ID_WQ + slot * 84, 0, 84, w, 8, L_X);
        {
          float s1 = 0.f, s2 = 0.f, s3 = 0.f, s4 = 0.f;
#pragma unroll
          for(int ks = 0; ks < 14; ++ks) {
            s1 = dot2(xf[ks][0], 0x3F803F80u, s1), s2 = dot2(xf[ks][0], xf[ks][0], s2);
            s3 = dot2(xf[ks][1], 0x3F803F80u, s3), s4 = dot2(xf[ks][1], xf[ks][1], s4);
            s1 = dot2(xf[ks][2], 0x3F803F80u, s1), s2 = dot2(xf[ks][2], xf[ks][2], s2);
            s3 = dot2(xf[ks][3], 0x3F803F80u, s3), s4 = dot2(xf[ks][3], xf[ks][3], s4);
          }
          s1 = gsum(s1 + s3), s2 = gsum(s2 + s4);
          A_FINISH_STATS();
        }
        if(w >= 3) {
          PREP_READ(0, false);
          PREP_READ(1, false);
        }
        asm volatile("s_waitcnt vmcnt(5)" ::: "memory");
        unsigned cv[14];
        if(w >= 3) {
          PREP_WRITE(0);
          PREP_WRITE(1);
        } else if(w < 2) {
#pragma unroll
          for(int ks = 0; ks < 14; ++ks)
            cv[ks] = __builtin_amdgcn_raw_buffer_load_b32(
                rws, ti < 96 ? OFF_CSP + (ID_WQ + slot * 84 + ks * 6 + (ti >> 4)) * 128 + (ti & 15) * 4 : OOB, 0, AUX_SC1);
          SB();
        }
        BARRIER();
        GEMM_PART(6, 2, 4, 0, 7, 4, STEP_NONE);
        if(ti < 96) {
          float csum = 0.f;
#pragma unroll
          for(int ks = 0; ks < 14; ++ks) csum += __uint_as_float(cv[ks]);
          *(LAS float *)(L + LC_CSQ + ti * 4) = csum;
        }
        if(w == 2) {
          u64 v = 0;
          for(unsigned spin = 0; spin < SPIN_CAP; ++spin) {
            v = __hip_atomic_load((const GAS u64 *)(ws + OFF_PREPF + (224 + (lane & 31)) * FS), __ATOMIC_RELAXED, __HIP_MEMORY_SCOPE_AGENT);
            if(__all((v | VOTE) == (tag | VOTE))) break;
            long_wait(spin);
          }
          const int ones = __popcll(__ballot(v != tag));
          if(lane == 0) *(LAS int *)(L + LS_SIDE) = ones > 32;
        }
        DRAIN();
        BARRIER();
        side = __builtin_amdgcn_readfirstlane(*(LAS int *)(L + LS_SIDE));
        if(ti == 0) set_word(ws, OFF_PREPG + b * 8, tag);
        GEMM_PART(6, 2, 4, 7, 14, 4, STEP_NONE);
      } else {
        const int xb = opq(PB(t, 2) + FO(F_X2 + w * 16));
        v2u sp[4];
        ROW_STATS(2);
#pragma unroll
        for(int ks = 0; ks < XRING; ++ks) XLOAD(ks);
        SB();
#define STEP_XA(ks) \
  if((ks) == 3) { \
    SUM_STATS(); \
    A_FINISH_STATS(); \
  } \
  STEP_X(ks)
        GEMM_PART(6, 2, 4, 0, 14, 4, STEP_XA);
      }
      const float qs = rs * (0.18898223650461362f * 1.4426950408889634f);
      f32x4 c[4];
#pragma unroll
      for(int n = 0; n < 4; ++n) c[n] = *(LAS f32x4 *)(L + LC_CSQ + n * 64 + g * 16);
      const float c4 = *(LAS float *)(L + LC_CSQ + 256 + li * 4), c5 = *(LAS float *)(L + LC_CSQ + 320 + li * 4);
      f32x4 a0 = (acc[2] - mu * c[2]) * rs, a1 = (acc[3] - mu * c[3]) * rs;
      const v4u kf = {pk2(a0[0], a0[1]), pk2(a0[2], a0[3]), g == 3 ? 0u : pk2(a1[0], a1[1]), g == 3 ? 0u : pk2(a1[2], a1[3])};
#pragma unroll
      for(int r = 0; r < 4; ++r) a0[r] = (acc[4][r] - mur[r] * c4) * rsr[r], a1[r] = (acc[5][r] - mur[r] * c5) * rsr[r];
      const unsigned vpad = li == 12 ? 0x3F803F80u : 0u;
      const v4u vf = {pk2(a0[0], a0[1]), pk2(a0[2], a0[3]), li >= 12 ? vpad : pk2(a1[0], a1[1]), li >= 12 ? vpad : pk2(a1[2], a1[3])};
      const int kvo = PB(t, layer) + FO(F_KV + (slot * 8 + w) * 2);
      st16(kf, rws, lane * 16, kvo);
      st16(vf, rws, lane * 16 + 1024, kvo);
      *(LAS v4u *)(L + L_KV + (16 + w) * 2048 + lane * 16) = kf;
      *(LAS v4u *)(L + L_KV + (16 + w) * 2048 + 1024 + lane * 16) = vf;
#define STEP_KVF(ks) \
  if((ks) == 9) { \
    DRAIN(); \
    if(lds_count(L + LS_KV, 1u, lane) == 8u * (layer + 1) - 1u) set_flag(ws, FLAG(t, layer, FL_KV, slot), lane, tag); \
    SB(); \
  }
      GEMM_PART(6, 0, 2, 0, 14, 4, STEP_KVF);
      a0 = (acc[0] - mu * c[0]) * qs, a1 = (acc[1] - mu * c[1]) * qs;
      qf = v4u{pk2(a0[0], a0[1]), pk2(a0[2], a0[3]), g == 3 ? 0x00003F80u : pk2(a1[0], a1[1]), g == 3 ? 0u : pk2(a1[2], a1[3])};
    }
    BARRIER();
    {
      IDS();
      if(layer == 0 && w == 6) {
        for(unsigned spin = 0; spin < SPIN_CAP; ++spin) {
          const v4u p0 = ld16(rws, OFF_PREPG + lane * 32, 0), p1 = ld16(rws, OFF_PREPG + lane * 32 + 16, 0);
          const unsigned tl = (unsigned)tag, th = (unsigned)(tag >> 32);
          if(__all(p0.x == tl && p0.z == tl && p1.x == tl && p1.z == tl && p0.y == th && p0.w == th && p1.y == th && p1.w == th)) break;
          __builtin_amdgcn_s_sleep(1);
          long_wait(spin);
          asm volatile("" ::: "memory");
        }
        __builtin_amdgcn_fence(__ATOMIC_ACQUIRE, "wavefront");
        if(b == 0 && lane == 0) set_word(ws, OFF_EPOCH, tag);
      }
      {
        const int ts = t - 2 + (w >> 2);
        if(ts >= 0) wait_flags(ws, FLAG(ts, layer, FL_KV, slot), tag);
#pragma unroll
        for(int i = 0; i < 4; ++i) {
          const int pc = w * 4 + i;
          if(ts >= 0) dma1k(rws, lane * 16, PB(ts, layer) + FO(F_KV + slot * 16 + (pc & 15)), ldsb + L_KV + pc * 1024);
          else *(LAS v4u *)(L + L_KV + pc * 1024 + lane * 16) = v4u{0u, 0u, (g == 3 && !(pc & 1)) ? 0x0000F149u : 0u, 0u};
        }
      }
      f32x4 c0, c16;
#pragma unroll
      for(int r = 0; r < 4; ++r) c0[r] = (4 * g + r > li) ? 0.f : -1e30f, c16[r] = (4 * g + r <= li) ? 0.f : -1e30f;
      DRAIN();
      BARRIER();
      if(layer == 0 && w == 0)
          asm volatile("s_mov_b32 m0, %0\n\ts_nop 0\n\tbuffer_load_dword %1, %2, %3 offen sc1 lds" ::"s"(ldsb + L_C + 1792),
                       "v"(lane < 10       ? FO(F_FL + lane * 2) + slot * FS
                           : slot >= 14 ? OOB
                           : lane < 26  ? FO(F_ST + (lane - 10) * 2) + slot * 128
                           : lane < 34  ? FO(F_ST + (16 + 4 * (slot >= 7) + ((lane - 26) >> 1)) * 2) + (2 * CSL(slot) + (lane & 1)) * 128
                                        : OOB),
                       "s"(rws),
                       "s"(PB(t, 2))
                       : "memory");
      LAS unsigned char *kv = L + L_KV + w * 2048 + lane * 16;
      const int wb = opq(ID_W2 + CSL(slot) * 224 + w * 28);
      f32x4 s[17];
      {
        v4u kf[17];
#pragma unroll
        for(int i = 0; i < 6; ++i) kf[i] = *(LAS v4u *)(kv + i * 2048);
        SB();
#pragma unroll
        for(int i = 0; i < 17; ++i) {
          if(i + 6 < 17) kf[i + 6] = *(LAS v4u *)(kv + (i + 6) * 2048);
          s[i] = mfma32(kf[i], qf, i == 0 ? c0 : i == 16 ? c16 : f32x4{0.f, 0.f, 0.f, 0.f});
          if((i & 3) == 1 && i < 16)
            dma1k(rws, lane * 16, WT(ID_WO + CS14(slot) * 32 + (i >> 2) * 8 + w), ldsb + L_WO + ((i >> 2) * 8 + w) * 1024);
          SB();
        }
      }
      v4u vf[17];
#pragma unroll
      for(int i = 0; i < 4; ++i) vf[i] = *(LAS v4u *)(kv + i * 2048 + 1024);
      float mx[4] = {-1e30f, -1e30f, -1e30f, -1e30f};
#pragma unroll
      for(int i = 0; i < 17; ++i) {
        mx[(2 * i) & 3] = fmaxf(fmaxf(mx[(2 * i) & 3], s[i][0]), s[i][1]);
        mx[(2 * i + 1) & 3] = fmaxf(fmaxf(mx[(2 * i + 1) & 3], s[i][2]), s[i][3]);
      }
      const float m = gmax(fmaxf(fmaxf(fmaxf(mx[0], mx[1]), mx[2]), mx[3]));
      f32x4 o0 = {0.f, 0.f, 0.f, 0.f}, o1 = {0.f, 0.f, 0.f, 0.f};
#pragma unroll
      for(int i = 0; i < 17; ++i) {
        if(i + 4 < 17) vf[i + 4] = *(LAS v4u *)(kv + (i + 4) * 2048 + 1024);
        const f32x4 d = s[i] - m;
        v2u p;
        p.x = pk2(__builtin_amdgcn_exp2f(d[0]), __builtin_amdgcn_exp2f(d[1]));
        p.y = pk2(__builtin_amdgcn_exp2f(d[2]), __builtin_amdgcn_exp2f(d[3]));
        o0 = mfma16(v2u{vf[i].x, vf[i].y}, p, o0);
        o1 = mfma16(v2u{vf[i].z, vf[i].w}, p, o1);
        if(i < 8 && i * 8 + w < W1_EARLY) dma1k(rws, lane * 16, WT(ID_W1 + slot * 98 + i * 8 + w), ldsb + L_X + (i * 8 + w) * 1024);
        if(layer == 0 && i >= 8 && i < 16) W2LOAD(i - 8);
        if(i & 1) SB();
      }
      const float inv = __builtin_amdgcn_rcpf(__shfl(o1[0], 48 + li));
      o0 *= inv, o1 *= inv;
      const v4u of = {pk2(o0[0], o0[1]), pk2(o0[2], o0[3]), pk2(o1[0], o1[1]), pk2(o1[2], o1[3])};
      st16(of, rws, lane * 16, PB(t, layer) + FO(F_O + w * 16 + slot));
      DRAIN();
      BARRIER();
      if(w == 7) set_flag(ws, FLAG(t, layer, FL_O, slot), lane, tag);
    }
    if(SLOT_OF(b0) < 14) {
      IDS();
      if(w == 7) wait_flags(ws, FLAG(t, layer, FL_O, li), tag);
      const rsrc_t rx = layer == 0 ? __builtin_amdgcn_make_buffer_rsrc((void *)x, 0, S * D * 2, 0x00020000) : rws;
      const v4u xr = ld16(rx, layer == 0 ? (16 * tt + li) * (D * 2) + g * 16 : lane * 16,
          layer == 0 ? slot * 64 : PB(t, 2) + FO(F_X2 + w * 16 + slot));
      BARRIER();
      v4u of[16];
      const int ob = opq(PB(t, layer) + FO(F_O + w * 16));
#pragma unroll
      for(int h = 0; h < 8; ++h) of[h] = ld16(rws, lane * 16 + (h & 3) * 1024, ob + X4(h));
      SB();
      v4u wf[32];
#pragma unroll
      for(int i = 0; i < 8; ++i) wf[i] = *(LAS v4u *)(L + L_WO + i * 1024 + lane * 16);
      SB();
      f32x4 a0 = {0.f, 0.f, 0.f, 0.f}, a1 = {0.f, 0.f, 0.f, 0.f};
#pragma unroll
      for(int h = 0; h < 16; ++h) {
        if(h < 8) of[h + 8] = ld16(rws, lane * 16 + (h & 3) * 1024, ob + X4(h + 8));
        if(h < 12)
          wf[2 * h + 8] = *(LAS v4u *)(L + L_WO + (2 * h + 8) * 1024 + lane * 16),
                     wf[2 * h + 9] = *(LAS v4u *)(L + L_WO + (2 * h + 9) * 1024 + lane * 16);
        a0 = mfma32(wf[2 * h], of[h], a0), a1 = mfma32(wf[2 * h + 1], of[h], a1);
        SB();
      }
      const v4u y = {pk2(a0[0] + bflo(xr.x), a0[1] + bfhi(xr.x)), pk2(a0[2] + bflo(xr.y), a0[3] + bfhi(xr.y)),
          pk2(a1[0] + bflo(xr.z), a1[1] + bfhi(xr.z)), pk2(a1[2] + bflo(xr.w), a1[3] + bfhi(xr.w))};
      st16(y, rws, lane * 16, PB(t, layer) + FO(F_X1 + w * 16 + slot));
      st8(piece_stats(y), rws, lane < 16 ? lane * 8 : OOB, PB(t, 2) + FO(F_ST + (layer * 8 + w) * 2) + slot * 128);
      DRAIN();
      BARRIER();
      if(w == 7) set_flag(ws, FLAG(t, layer, FL_X1, slot), lane, tag);
    }
    {
      IDS();
      const int xb = opq(PB(t, layer) + FO(F_X1 + w * 16));
      if(w == 7) wait_flags(ws, FLAG(t, layer, FL_X1, (li < 14 ? li : 0)), tag);
      if(layer == 0 && ti < 112) {
        const int nt = ti >> 4, i = ti & 15;
        float sum = 0.f;
#pragma unroll
        for(int ks = 0; ks < 14; ++ks)
          sum += __uint_as_float(__builtin_amdgcn_raw_buffer_load_b32(rws, OFF_CSP + (ID_W1 + slot * 98 + ks * 7 + nt) * 128 + i * 4, 0, AUX_SC1));
        *(LAS float *)(L + LC_CS1 + ti * 4) = sum;
        const int col = nt < 6 ? 112 * slot + 32 * (nt >> 1) + 8 * (i >> 2) + (i & 3) + 4 * (nt & 1) : 112 * slot + 96 + i;
        *(LAS float *)(L + LC_B1 + ti * 4) = bflo(b1[col]);
      }
      BARRIER();
      v4u xf[14];
      v2u sp[4];
      ROW_STATS(layer);
#pragma unroll
      for(int ks = 0; ks < XRING; ++ks) XLOAD(ks);
      SB();
      WT_DMA(ID_W1 + slot * 98, W1_EARLY, 98, w, 8, L_X);
      f32x4 acc[7];
#pragma unroll
      for(int n = 0; n < 7; ++n) acc[n] = f32x4{0.f, 0.f, 0.f, 0.f};
      float rs, nmr;
      const int wb = opq(ID_W2 + CSL(slot) * 224 + w * 28);
      SB();
#define STEP_B(ks) \
  if((ks) == 3) {   \
    SUM_STATS(); \
    const float mu = s1 * (1.f / D), var = fmaxf(s2 * (1.f / D) - mu * mu, 0.f), r1 = __builtin_amdgcn_rsqf(var + EPS); \
    rs = r1 * __builtin_amdgcn_rsqf(var * r1 * r1 + EPS), nmr = -mu * rs; \
    asm volatile("" : "+v"(rs), "+v"(nmr)); \
  } \
  STEP_X(ks); \
  if(layer == 0 && (ks) >= 6) { \
    _Pragma("unroll") for(int j = 8 + ((ks)-6) * 3; j < 8 + ((ks)-5) * 3 && j < 28; ++j) W2LOAD(j); \
  }
      GEMM_PART(7, 0, 7, 0, 9, 7, STEP_B);
      asm volatile("s_waitcnt vmcnt(%0)" ::"i"(14 - XRING) : "memory");
      BARRIER();
      GEMM_PART(7, 0, 7, 9, 14, 7, STEP_B);
      const int ho = PB(t, layer) + FO(F_H + w * 64 + (slot >> 1) * 8);
      f32x4 cc[7], bb[7];
#pragma unroll
      for(int n = 0; n < 7; ++n) cc[n] = *(LAS f32x4 *)(L + LC_CS1 + n * 64 + g * 16), bb[n] = *(LAS f32x4 *)(L + LC_B1 + n * 64 + g * 16);
      SB();
      unsigned hp[14];
#pragma unroll
      for(int n = 0; n < 7; ++n) {
        const f32x4 v = acc[n] * rs + (cc[n] * nmr + bb[n]);
        const f32x4 a = v * (v * v * -0.1029432396f + -2.3022081986f);
        const f32x4 e = {__builtin_amdgcn_exp2f(a[0]), __builtin_amdgcn_exp2f(a[1]), __builtin_amdgcn_exp2f(a[2]), __builtin_amdgcn_exp2f(a[3])};
        const f32x4 d = e + 1.f;
        const f32x4 r = {__builtin_amdgcn_rcpf(d[0]), __builtin_amdgcn_rcpf(d[1]), __builtin_amdgcn_rcpf(d[2]), __builtin_amdgcn_rcpf(d[3])};
        const f32x4 hv = v * r;
        hp[2 * n] = pk2(hv[0], hv[1]), hp[2 * n + 1] = pk2(hv[2], hv[3]);
        if(n & 1)
          st16(v4u{hp[2 * n - 2], hp[2 * n - 1], hp[2 * n], hp[2 * n + 1]}, rws, lane * 16,
              ho + (n < 4 ? 2 * (slot & 1) + (n >> 1) : 4 + (slot & 1)) * 1024);
        if(n == 6) st8(v2u{hp[12], hp[13]}, rws, lane * 8, ho + 6144 + (slot & 1) * 512);
      }
      DRAIN();
      BARRIER();
      if(w == 7) {
        set_flag(ws, FLAG(t, layer, FL_H, slot), lane, tag);
      } else if(layer == 0 && slot < 14) {
        WT_DMA(ID_WQ + slot * 84, 0, WQ_HOP, w, 7, L_X);
      }
    }
    if(SLOT_OF(b0) < 14) {
      IDS();
      const int csl = CSL(slot), chalf = slot >= 7;
      wait_flags(ws, FLAG(t, layer, FL_H, 2 * w + (lane & 1)), tag);
      const int ho = opq(PB(t, layer) + FO(F_H + 4 * chalf * 64 + w * 8));
      v4u hf[8];
#pragma unroll
      for(int i = 0; i < 8; ++i) hf[i] = ld16(rws, lane * 16 + (i >> 2) * 1024, ho + (i & 3) * FO(64));
      const int ow = 4 * chalf + (w >> 1), oks = 2 * csl + (w & 1);
      v4u xr, bb;
      SB();
      f32x4 acc[4][4];
#pragma unroll
      for(int q = 0; q < 4; ++q)
#pragma unroll
        for(int n = 0; n < 4; ++n) acc[q][n] = f32x4{0.f, 0.f, 0.f, 0.f};
#pragma unroll
      for(int k = 0; k < 7; ++k) {
#pragma unroll
        for(int q = 0; q < 4; ++q) {
          const v4u cur = hf[(k & 1) * 4 + q];
          if(k == 4) {
            const v2u u0 = ld8(rws, lane * 8, ho + q * FO(64) + 6144), u1 = ld8(rws, lane * 8 + 512, ho + q * FO(64) + 6144);
            hf[q] = v4u{u0.x, u0.y, u1.x, u1.y};
          } else if(k + 2 < 7)
            hf[(k & 1) * 4 + q] = ld16(rws, lane * 16 + (k & 3) * 1024, ho + q * FO(64) + 2048 + (k >> 2) * 4096);
          if(k == 5 && q == 1) xr = ld16(rws, lane * 16, opq(PB(t, layer) + FO(F_X1 + ow * 16 + oks)));
          if(k == 5 && q == 2)
            bb = __builtin_amdgcn_raw_buffer_load_b128(__builtin_amdgcn_make_buffer_rsrc((void *)b2, 0, D * 2, 0x00020000), g * 16, opq(oks * 64), 0);
#pragma unroll
          for(int n = 0; n < 4; ++n) acc[q][n] = mfma32(w2f[k * 4 + n], cur, acc[q][n]);
          SB();
        }
      }
      if(layer == 0) WT_DMA(ID_WQ + slot * 84, WQ_HOP, 84, w, 8, L_X);
      f32x4 a0, a1;
#pragma unroll
      for(int f = 0; f < 8; ++f) {
        const f32x4 p0 = acc[f >> 1][2 * (f & 1)], p1 = acc[f >> 1][2 * (f & 1) + 1];
        if(f == w) a0 = p0, a1 = p1;
        else
          *(LAS v4u *)(L + L_RED + (f * 7 + (w < f ? w : w - 1)) * 1024 + lane * 16) =
              v4u{pk2(p0[0], p0[1]), pk2(p0[2], p0[3]), pk2(p1[0], p1[1]), pk2(p1[2], p1[3])};
      }
      BARRIER();
      {
        const unsigned one = g == (li >> 2) ? ((li & 1) ? 0x3F800000u : 0x00003F80u) : 0u;
        const v4u e0 = {(li & 2) ? 0u : one, (li & 2) ? one : 0u, 0u, 0u}, e1 = {0u, 0u, (li & 2) ? 0u : one, (li & 2) ? one : 0u};
        v4u p[7];
#pragma unroll
        for(int s = 0; s < 7; ++s) p[s] = *(LAS v4u *)(L + L_RED + (w * 7 + s) * 1024 + lane * 16);
        a0 = mfma32(e0, xr, a0), a1 = mfma32(e1, xr, a1);
        a0 = mfma32(e0, bb, a0), a1 = mfma32(e1, bb, a1);
#pragma unroll
        for(int s = 0; s < 7; ++s) a0 = mfma32(e0, p[s], a0), a1 = mfma32(e1, p[s], a1);
      }
      const v4u y = {pk2(a0[0], a0[1]), pk2(a0[2], a0[3]), pk2(a1[0], a1[1]), pk2(a1[2], a1[3])};
      if(layer == 0) {
        st16(y, rws, lane * 16, PB(t, 2) + FO(F_X2 + ow * 16 + oks));
        st8(piece_stats(y), rws, lane < 16 ? lane * 8 : OOB, PB(t, 2) + FO(F_ST + (16 + ow) * 2) + oks * 128);
      } else {
        *(GAS v4u *)(out + (16 * (8 * t + ow) + li) * D + 32 * oks + 8 * g) = y;
      }
    }
    if(layer == 0) {
      IDS();
      if(slot >= 14) WT_DMA(ID_WQ + slot * 84, 0, 84, w, 8, L_X);
      DRAIN();
      BARRIER();
      if(w == 7) {
        if(slot < 14) set_flag(ws, FLAG(t, 0, FL_X2, slot), lane, tag);
        wait_flags(ws, FLAG(t, 0, FL_X2, (li < 14 ? li : 0)), tag);
      }
      BARRIER();
    }
  };
  run_layer(IntC<0>{});
  run_layer(IntC<1>{});
  asm volatile(".Lfk_end:");
}
}

extern "C" void kernel_launch(void *const *d_in, const int *in_sizes, int n_in, void *d_out, int out_size, void *d_ws, size_t ws_size,
    hipStream_t stream) {
  const size_t pad = (size_t)(-(intptr_t)d_ws) & (size_t)0x1FFFFF;
  fused_kernel<<<NWG, NTHR, 0, stream>>>((const u16 *)d_in[0], (const u16 *)d_in[1], (const u16 *)d_in[2], (const u16 *)d_in[4],
      (const u16 *)d_in[5], (const u16 *)d_in[6], (const u16 *)d_in[7], (u16 *)d_out, (unsigned char *)d_ws + pad);
}
```
